```python
import torch

def _softmax(x, axis=-1):
    return torch.softmax(x, dim=axis)
def _gelu(x):
    return torch.nn.functional.gelu(x, approximate="tanh")
def _silu(x):
    return torch.nn.functional.silu(x)
def _sigmoid(x):
    return torch.sigmoid(x)
def _layernorm(x, axis=-1, eps=1e-5):
    m = x.mean(dim=axis, keepdim=True)
    v = x.var(dim=axis, keepdim=True, unbiased=False)
    return (x - m) / torch.sqrt(v + eps)
def _scatter_add(tgt, idx, src, axis=0):
    return tgt.clone().index_add_(axis, idx, src)
def _segment_sum(src, seg, n):
    out = torch.zeros((n,) + src.shape[1:], dtype=src.dtype)
    return out.index_add_(0, seg, src)
def _pad(x, pw):
    flat = tuple(v for pair in reversed(pw) for v in pair)
    return torch.nn.functional.pad(x, flat)

kernel_name = 'parallel_sum__softmax_full_mha__glu_gated_swiglu__prompt'

def setup_inputs():

    _w = (lambda a: (lambda t: t.to(torch.bfloat16) if t.is_floating_point() else t)(torch.as_tensor(a)))
    def _A_setup():
        B, S, D = 1, 2048, 512
        return {
            "x": _w((lambda *s: torch.randn(*s, dtype=torch.bfloat16) * 0.05)(B, S, D)),
            "wqkv": _w((lambda *s: torch.randn(*s, dtype=torch.bfloat16) * 0.05)(D, 3 * D)),
            "wo": _w((lambda *s: torch.randn(*s, dtype=torch.bfloat16) * 0.05)(D, D)),
        }
    _A_in = _A_setup()

    def _B_setup():
        B, S, D, F = 1, 2048, 512, 1792
        return {
            "x": _w((lambda *s: torch.randn(*s, dtype=torch.bfloat16) * 0.05)(B, S, D)),
            "w1": _w((lambda *s: torch.randn(*s, dtype=torch.bfloat16) * 0.4)(D, F) * (2.0 / D) ** 0.5),
            "w3": _w((lambda *s: torch.randn(*s, dtype=torch.bfloat16) * 0.4)(D, F) * (2.0 / D) ** 0.5),
            "w2": _w((lambda *s: torch.randn(*s, dtype=torch.bfloat16) * 0.4)(F, D) * (2.0 / F) ** 0.5),
        }
    _B_in = _B_setup()

    return {
        "A_x": _A_in["x"],
        "A_wqkv": _A_in["wqkv"],
        "A_wo": _A_in["wo"],
        "B_x": _B_in["x"],
        "B_w1": _B_in["w1"],
        "B_w3": _B_in["w3"],
        "B_w2": _B_in["w2"],
    }

def reference(A_x, A_wqkv, A_wo, B_x, B_w1, B_w3, B_w2):
    def _A_ref(x, wqkv, wo):
        B, S, D = x.shape
        H = 32; Dh = D // H
        qkv = (lambda x, *d: x.permute(*d))((x @ wqkv).reshape(B, S, 3, H, Dh), 2, 0, 3, 1, 4)
        q, k, v = qkv[0], qkv[1], qkv[2]
        scale = Dh ** -0.5
        scores = torch.einsum("bhqd,bhkd->bhqk", q, k) * scale
        p = _softmax(scores, axis=-1)
        o = torch.einsum("bhqk,bhkd->bhqd", p, v)
        return (lambda x, *d: x.permute(*d))(o, 0, 2, 1, 3).reshape(B, S, D) @ wo

    def _B_ref(x, w1, w3, w2):
        gate = torch.einsum("bsd,df->bsf", x, w1)
        up = torch.einsum("bsd,df->bsf", x, w3)
        h = _silu(gate) * up
        out = torch.einsum("bsf,fd->bsd", h, w2)
        return out

    x = A_x
    xn = _layernorm(x, axis=-1)
    return x + _A_ref(xn, A_wqkv, A_wo) + _B_ref(xn, B_w1, B_w3, B_w2)
```

```cpp
#include <hip/hip_runtime.h>
#include <hip/hip_bf16.h>
#include <stdint.h>
typedef __hip_bfloat16 bf16;
static __device__ __forceinline__ float b2f(bf16 x){ return __bfloat162float(x);}
static __device__ __forceinline__ float rb(float x){ return __bfloat162float(__float2bfloat16(x));}
#define S 2048
#define D 512
#define F 1792
#define H 32
#define DH 16
__global__ void ln_k(const bf16* x, bf16* xn){
  int r=blockIdx.x; int t=threadIdx.x;
  float v[8]; float s=0;
  for(int i=0;i<8;i++){v[i]=b2f(x[r*D+t*8+i]); s+=v[i];}
  for(int o=32;o>0;o>>=1) s+=__shfl_xor(s,o);
  float m=s/D; float q=0;
  for(int i=0;i<8;i++){float d=v[i]-m; q+=d*d;}
  for(int o=32;o>0;o>>=1) q+=__shfl_xor(q,o);
  float var=rb(q/D); float mb=rb(m);
  float den=rb(sqrtf(rb(var+1e-5f)));
  for(int i=0;i<8;i++){ xn[r*D+t*8+i]=__float2bfloat16(rb(v[i]-mb)/den);}
}
__global__ void gemm_k(const bf16* A,const bf16* B,bf16* C,int M,int N,int K){
  int n=blockIdx.x*64+threadIdx.x; int m=blockIdx.y;
  float acc=0; for(int k=0;k<K;k++) acc+=b2f(A[m*K+k])*b2f(B[k*N+n]);
  C[m*N+n]=__float2bfloat16(acc);
}
__global__ void attn_k(const bf16* qkv, bf16* o){
  int q=blockIdx.x*64+threadIdx.x; int h=blockIdx.y;
  float qv[16]; for(int d=0;d<16;d++) qv[d]=b2f(qkv[q*1536+h*16+d]);
  float m=-1e30f;
  for(int k=0;k<S;k++){ float s=0; for(int d=0;d<16;d++) s+=qv[d]*b2f(qkv[k*1536+512+h*16+d]); s=rb(s)*0.25f; m=fmaxf(m,s);}
  float l=0; float acc[16]; for(int d=0;d<16;d++) acc[d]=0;
  for(int k=0;k<S;k++){ float s=0; for(int d=0;d<16;d++) s+=qv[d]*b2f(qkv[k*1536+512+h*16+d]); s=rb(s)*0.25f; float p=__expf(s-m); l+=p; for(int d=0;d<16;d++) acc[d]+=p*b2f(qkv[k*1536+1024+h*16+d]);}
  for(int d=0;d<16;d++) o[q*512+h*16+d]=__float2bfloat16(acc[d]/l);
}
__global__ void silu_k(const bf16* g,const bf16* u,bf16* h,int n){
  int i=blockIdx.x*256+threadIdx.x; if(i<n){ float a=b2f(g[i]); float s=rb(a/(1.f+__expf(-a))); h[i]=__float2bfloat16(s*b2f(u[i])); }
}
__global__ void fin_k(const bf16* x,const bf16* a,const bf16* b,bf16* o,int n){
  int i=blockIdx.x*256+threadIdx.x; if(i<n){ o[i]=__float2bfloat16(rb(b2f(x[i])+b2f(a[i]))+b2f(b[i])); }
}
extern "C" void kernel_launch(void* const* d_in, const int* in_sizes, int n_in,
                              void* d_out, int out_size, void* d_ws, size_t ws_size,
                              hipStream_t stream) {
  const bf16* x=(const bf16*)d_in[0]; const bf16* wqkv=(const bf16*)d_in[1]; const bf16* wo=(const bf16*)d_in[2];
  const bf16* w1=(const bf16*)d_in[4]; const bf16* w3=(const bf16*)d_in[5]; const bf16* w2=(const bf16*)d_in[6];
  bf16* ws=(bf16*)d_ws;
  bf16* xn=ws; bf16* qkv=xn+S*D; bf16* o=qkv+S*1536; bf16* a=o+S*D; bf16* g=a+S*D; bf16* u=g+S*F; bf16* h=u+S*F; bf16* b=h+S*F;
  ln_k<<<S,64,0,stream>>>(x,xn);
  gemm_k<<<dim3(1536/64,S),64,0,stream>>>(xn,wqkv,qkv,S,1536,D);
  attn_k<<<dim3(S/64,H),64,0,stream>>>(qkv,o);
  gemm_k<<<dim3(D/64,S),64,0,stream>>>(o,wo,a,S,D,D);
  gemm_k<<<dim3(F/64,S),64,0,stream>>>(xn,w1,g,S,F,D);
  gemm_k<<<dim3(F/64,S),64,0,stream>>>(xn,w3,u,S,F,D);
  silu_k<<<S*F/256,256,0,stream>>>(g,u,h,S*F);
  gemm_k<<<dim3(D/64,S),64,0,stream>>>(h,w2,b,S,D,F);
  fin_k<<<S*D/256,256,0,stream>>>(x,a,b,(bf16*)d_out,S*D);
}
```

```cpp
#include <hip/hip_runtime.h>
#include <stdint.h>

typedef float f32x16 __attribute__((ext_vector_type(16)));
typedef float f32x8 __attribute__((ext_vector_type(8)));
typedef float f32x4 __attribute__((ext_vector_type(4)));
typedef unsigned u32x4 __attribute__((ext_vector_type(4)));
typedef unsigned u32x2 __attribute__((ext_vector_type(2)));
typedef unsigned u32x8 __attribute__((ext_vector_type(8)));
typedef unsigned u32x16 __attribute__((ext_vector_type(16)));
typedef __bf16 bf16x2 __attribute__((ext_vector_type(2)));
typedef __bf16 bf16x8 __attribute__((ext_vector_type(8)));
typedef __bf16 bf16x16 __attribute__((ext_vector_type(16)));
typedef short s16x4 __attribute__((ext_vector_type(4)));
typedef unsigned long long u64;
#define LAS __attribute__((address_space(3)))
#define GAS __attribute__((address_space(1)))
#define DI __device__ __forceinline__

constexpr unsigned WS_EPOCH = 0, WS_FQ = 4096, WS_FH = WS_FQ + 3 * 32 * 16 * 128, WS_FO = WS_FH + 64 * 16 * 128, WS_ST = WS_FO + 32 * 32 * 128, WS_CS = 1u << 19;
constexpr unsigned WS_Q = 1u << 20, WS_KV = 3u << 20, WS_H = 7u << 20, WS_O = 14u << 20, WS_DBG = 16u << 20;
constexpr unsigned L_X = 0, L_R = 131072, L_M = 159744, L_P = L_M, L_QC = L_M + 16, L_AP = L_M + 32, L_RS = L_M + 64, L_MN = L_RS + 512, L_CX = L_MN + 512, L_CS = L_CX + 1536, L_DUM = L_CS + 896, L_DBG = L_DUM + 256;
constexpr unsigned L_WO = 65536;
constexpr unsigned SPIN_CAP = 1u << 20;
constexpr float LOG2E = 1.4426950408889634f;

#define STAMP(n) do { } while (0)
#define LST(i) do { } while (0)
#define LDUMP() do { } while (0)

DI f32x16 mfma(u32x4 a, u32x4 b, f32x16 c) { return __builtin_amdgcn_mfma_f32_32x32x16_bf16(__builtin_bit_cast(bf16x8, a), __builtin_bit_cast(bf16x8, b), c, 0, 0, 0); }
DI unsigned pk(float lo, float hi) { bf16x2 v = {(__bf16)lo, (__bf16)hi}; return __builtin_bit_cast(unsigned, v); }
DI float rb(float x) { return (float)(__bf16)x; }
DI f32x4 mfma4(unsigned a0, unsigned a1, unsigned b0, unsigned b1, f32x4 c) {
    return __builtin_amdgcn_mfma_f32_4x4x4bf16_1k(__builtin_bit_cast(s16x4, (u32x2){a0, a1}), __builtin_bit_cast(s16x4, (u32x2){b0, b1}), c, 0, 0, 0); }
constexpr unsigned ONE2 = 0x3f803f80u;
DI u32x4 ldsr128(unsigned a) { return *(const LAS u32x4*)(size_t)a; }
DI void ldsw128(unsigned a, u32x4 v) { *(LAS u32x4*)(size_t)a = v; }
DI u32x4 ldtr(unsigned a0, unsigned a1) {
    u32x2 lo = __builtin_bit_cast(u32x2, __builtin_amdgcn_ds_read_tr16_b64_v4i16((LAS s16x4*)(size_t)a0));
    u32x2 hi = __builtin_bit_cast(u32x2, __builtin_amdgcn_ds_read_tr16_b64_v4i16((LAS s16x4*)(size_t)a1));
    return (u32x4){lo[0], lo[1], hi[0], hi[1]};
}
DI float xhalf_sum(float v) { auto rr = __builtin_amdgcn_permlane32_swap(__float_as_uint(v), __float_as_uint(v), false, false); return __uint_as_float(rr[0]) + __uint_as_float(rr[1]); }
DI float xhalf_max(float v) { auto rr = __builtin_amdgcn_permlane32_swap(__float_as_uint(v), __float_as_uint(v), false, false); return __builtin_fmaxf(__uint_as_float(rr[0]), __uint_as_float(rr[1])); }
template <int C> DI float dpp_add(float v) { return v + __uint_as_float(__builtin_amdgcn_update_dpp(0u, __float_as_uint(v), C, 0xf, 0xf, false)); }
DI float wave_sum(float v) {
    v = dpp_add<0x121>(v); v = dpp_add<0x122>(v); v = dpp_add<0x124>(v); v = dpp_add<0x128>(v);
    auto a = __builtin_amdgcn_permlane16_swap(__float_as_uint(v), __float_as_uint(v), false, false); v = __uint_as_float(a[0]) + __uint_as_float(a[1]);
    return xhalf_sum(v); }
DI float lsel(float a, float b, u64 m) { float d; asm("v_cndmask_b32 %0, %1, %2, %3" : "=v"(d) : "v"(a), "v"(b), "s"(m)); return d; }
#define LO(p) ((unsigned)(size_t)(p))
#define HI(p) ((unsigned)((size_t)(p) >> 32))
#define FENCE() asm volatile("" ::: "memory")
#define WAITV(n) asm volatile("s_waitcnt vmcnt(" #n ")" ::: "memory")
#define BARRIER() asm volatile("s_waitcnt lgkmcnt(0)\n\ts_barrier" ::: "memory")
#define GLDS(m0v, voff, sbase, off) asm volatile("s_mov_b32 m0, %0\n\ts_nop 0\n\tglobal_load_lds_dwordx4 %1, %2 offset:%c3" :: "s"(m0v), "v"(voff), "s"(sbase), "i"(off) : "memory")
#define ST16(v, voff, sbase, off) asm volatile("global_store_dwordx4 %0, %1, %2 offset:%c3 sc1\n\ts_nop 1" :: "v"(voff), "v"(v), "s"(sbase), "i"(off) : "memory")
#define LD16(v, voff, sbase, off) asm volatile("global_load_dwordx4 %0, %1, %2 offset:%c3 sc1" : "=v"(v) : "v"(voff), "s"(sbase), "i"(off) : "memory")

DI void poll_flags(const char* base, unsigned voff, u64 ep) {
    u64 v; unsigned c;
    asm volatile("s_mov_b32 %1, 0x40000\n"
                 "1:\n\tglobal_load_dwordx2 %0, %2, %3 sc1\n\ts_waitcnt vmcnt(0)\n\tv_cmp_ne_u64 vcc, %4, %0\n\ts_cbranch_vccz 2f\n\ts_sleep 1\n\ts_sub_u32 %1, %1, 1\n\ts_cbranch_scc0 1b\n2:"
                 : "=&v"(v), "=&s"(c) : "v"(voff), "s"(base), "s"(ep) : "memory", "vcc", "scc");
}
DI void set_flag(char* p, u64 ep) { __hip_atomic_store((GAS u64*)p, ep, __ATOMIC_RELAXED, __HIP_MEMORY_SCOPE_AGENT); }
DI void lds_post(unsigned a, unsigned inc) { asm volatile("s_waitcnt lgkmcnt(0)\n\ts_mov_b64 exec, 1\n\tds_add_u32 %0, %1\n\ts_mov_b64 exec, -1" :: "v"(a), "v"(inc) : "memory"); }
DI void wait_b(unsigned a, unsigned want4) {
    unsigned t, v, c;
    asm volatile("s_mov_b32 %2, 0x400000\n"
                 "1:\n\tds_read_b32 %1, %3\n\ts_waitcnt lgkmcnt(0)\n\tv_readfirstlane_b32 %0, %1\n\ts_sub_u32 %2, %2, 1\n\ts_cbranch_scc1 2f\n\ts_or_b32 %0, %0, 0x80808080\n\ts_sub_u32 %0, %0, %4\n\t"
                 "s_andn2_b32 %0, 0x80808080, %0\n\ts_cbranch_scc0 2f\n\ts_sleep 1\n\ts_branch 1b\n2:"
                 : "=&s"(t), "=&v"(v), "=&s"(c) : "v"(a), "s"(want4) : "memory", "scc");
}
#define ALL4(v) ((unsigned)(v) * 0x01010101u)
#define OPAQUE(x) asm volatile("" : "+v"(x))
#define FORCE_SLOW 0
constexpr int CODE_BYTES = 32000;

#define P1_OPERANDS : "+{s[80:87]}"(sg) : "{v[128:143]}"(vi0), "{v[144:159]}"(vi1), "{s[64:79]}"(si), "{s[88:89]}"(ep) : "memory", "scc", "vcc", "v16", "v17", "v18", "v19", "v20", "v21", "v22", "v23", "v24", "v25", "v26", "v27", "v28", "v29", "v30", "v31", "v32", "v33", "v34", "v35", "v36", "v37", "v38", "v39", "v40", "v41", "v42", "v43", "v44", "v45", "v46", "v47", "v48", "v49", "v50", "v51", "v52", "v53", "v54", "v55", "v56", "v57", "v58", "v59", "v60", "v61", "v62", "v63", "v64", "v65", "v66", "v67", "v68", "v69", "v70", "v71", "v72", "v73", "v74", "v75", "v76", "v77", "v78", "v79", "v80", "v81", "v82", "v83", "v84", "v85", "v86", "v87", "v88", "v89", "v90", "v91", "v92", "v93", "v94", "v95", "v96", "v97", "v98", "v99", "v100", "v101", "v102", "v103", "v104", "v105", "v106", "v107", "v108", "v109", "v110", "v111", "v112", "v113", "v114", "v115", "v116", "v117", "v118", "v119", "v120", "v121", "v122", "v123", "v124", "v125", "v126", "v127", "v160", "v161", "v162", "v163", "v164", "v165", "v166", "v167", "v168", "v169", "v170", "v171", "v172", "v173", "v174", "v175", "v176", "v177", "v178", "v179", "v180", "v181", "v182", "v183", "s50", "s51", "s52", "s53", "s54", "s55", "s56", "s57", "s58", "s59", "s60", "s61", "s62", "s63"
DI u32x4 cs_frag(float cs) { const float hi = rb(cs); return (u32x4){pk(hi, cs - hi), 0u, 0u, 0u}; }
DI u32x4 pack8(const f32x16& a, int o) { return (u32x4){pk(a[o], a[o + 1]), pk(a[o + 2], a[o + 3]), pk(a[o + 4], a[o + 5]), pk(a[o + 6], a[o + 7])}; }
DI void attn_slow(const char* ws, int h, int tile, int lane, unsigned vone, f32x16& o) {
    const int r = lane & 31, hh = lane >> 5;
    const char* qb = ws + WS_Q + (unsigned)(h * 64 + tile) * 1024u; const char* kb = ws + WS_KV + (unsigned)h * 131072u;
    unsigned ok = lane * 16, ov = hh * 256 + (r & 15) * 16;
    u32x4 q, k, v0, v1; LD16(q, ok, qb, 0);
    float m = -__builtin_inff(); const f32x16 z = {};
#pragma unroll 1
    for (int t = 0; t < 64; ++t) {
        const unsigned a = ok + t * 2048; LD16(k, a, kb, 0);
        asm volatile("s_waitcnt vmcnt(0)" : "+v"(k), "+v"(q) :: "memory");
        const f32x16 s = mfma(k, q, z);
#pragma unroll
        for (int e = 0; e < 16; ++e) m = __builtin_fmaxf(m, s[e]);
    }
    m = xhalf_max(m);
    f32x16 c;
#pragma unroll
    for (int e = 0; e < 16; ++e) { c[e] = -m; o[e] = 0.f; }
#pragma unroll 1
    for (int t = 0; t < 64; ++t) {
        const unsigned a = ok + t * 2048, b = ov + t * 2048; LD16(k, a, kb, 0); LD16(v0, b, kb, 1024); LD16(v1, b, kb, 1536);
        asm volatile("s_waitcnt vmcnt(0)" : "+v"(k), "+v"(v0), "+v"(v1) :: "memory");
        if (r >= 16) { v0 = (u32x4){vone, vone, vone, vone}; v1 = v0; }
        f32x16 s = mfma(k, q, c);
#pragma unroll
        for (int e = 0; e < 16; ++e) s[e] = __builtin_amdgcn_exp2f(s[e]);
        const f32x8 lo = __builtin_shufflevector(s, s, 0, 1, 2, 3, 4, 5, 6, 7), hi = __builtin_shufflevector(s, s, 8, 9, 10, 11, 12, 13, 14, 15);
        o = __builtin_amdgcn_smfmac_f32_32x32x32_bf16(__builtin_bit_cast(bf16x8, v0), __builtin_bit_cast(bf16x16, lo), o, (int)0xdddddddd, 0, 0);
        o = __builtin_amdgcn_smfmac_f32_32x32x32_bf16(__builtin_bit_cast(bf16x8, v1), __builtin_bit_cast(bf16x16, hi), o, (int)0xdddddddd, 0, 0);
    }
}

__global__ __launch_bounds__(512, 2) void fused_block(const char* __restrict__ x, const char* __restrict__ wqkv, const char* __restrict__ wo, const char* __restrict__ w1,
                                                      const char* __restrict__ w3, const char* __restrict__ w2, char* __restrict__ out, char* __restrict__ ws) {
    __shared__ __attribute__((aligned(1024))) char lds[163840];
    asm volatile("" :: "s"((unsigned)(size_t)(LAS char*)lds), "s"(x), "s"(wqkv), "s"(wo), "s"(w1), "s"(w3), "s"(w2), "s"(out), "s"(ws));
    const int tid = threadIdx.x, lane = tid & 63, w = __builtin_amdgcn_readfirstlane(tid >> 6), r = lane & 31, hh = lane >> 5;
    const int xcd = blockIdx.x & 7, y = blockIdx.x >> 3;
    const int bi = 8 * (xcd & 1) + (y & 7), bj = 4 * (xcd >> 1) + (y >> 3);
    const bool isA = w < 4; const int g = w & 3;
    STAMP(0); LST(0);
    unsigned sv[4], m0s; const char* sb;
    u32x4 xrow; u32x2 epv;
    { const unsigned vo = lane * 16, z = 0; const char* xr = x + (unsigned)(128 * bi + 8 * bj + w) * 1024u;
      asm volatile("global_load_dwordx4 %0, %2, %3\n\tglobal_load_dwordx2 %1, %4, %5 sc1" : "=&v"(xrow), "=&v"(epv) : "v"(vo), "s"(xr), "v"(z), "s"(ws + WS_EPOCH) : "memory"); }
    const int rot = ((y & 7) + 2 * (y >> 3)) & 7;
    if (w == 3) {
        u64 pc; asm volatile("s_getpc_b64 %0" : "=s"(pc));
        const unsigned vo = min((unsigned)((y & 7) * 40 + min(lane, 39)) * 128u, (unsigned)CODE_BYTES);
        asm volatile("s_mov_b32 m0, %0\n\ts_nop 0\n\tglobal_load_lds_dword %1, %2" :: "s"(L_DUM), "v"(vo), "s"(pc) : "memory");
    }
    if (isA) {
        sb = wqkv + bj * 192; m0s = L_R + g * 1024;
#pragma unroll
        for (int m = 0; m < 3; ++m) { const unsigned U = tid + 256 * m; sv[m] = (U / 12) * 3072 + (U % 12) * 16; }
        sv[3] = L_R + tid * 16;
#pragma unroll
        for (int m = 0; m < 3; ++m) GLDS(m0s + m * 4096, sv[m], sb + rot * 196608, 0);

    } else {
        sb = x + (unsigned)bi * 131072u; m0s = L_X + g * 4096;
#pragma unroll
        for (int m = 0; m < 4; ++m) sv[m] = (32 * g + 8 * m + (lane >> 3)) * 1024 + (((lane & 7) ^ (4 * (m & 1) + (lane >> 4))) << 4);
#pragma unroll
        for (int m = 0; m < 4; ++m) GLDS(m0s + rot * 16384 + m * 1024, sv[m], sb + rot * 128, 0);
    }
    LST(1);
    if (tid < 16) *(LAS unsigned*)(size_t)(L_M + tid * 4) = 0;
    const int tq = (lane & 15) >> 2, tp = lane & 3, nb = (lane >> 4) & 1;
    const unsigned sel = (lane & 15) == ((lane >> 4) & 1) ? ONE2 : 0u;
    const unsigned mb13 = L_R + g * 8192;
    const unsigned v1 = g < 3 ? (lane >> 2) * 3584 + bj * 224 + g * 64 + (lane & 3) * 16 : ((lane & 31) >> 1) * 3584 + bj * 224 + 192 + (lane & 1) * 16;
    const unsigned v3 = g < 3 ? v1 + ((lane & 2) ? -32 : 32) : v1 + (((lane >> 3) & 1) ? -4 * 3584 : 4 * 3584);
    u64 ep; u32x8 sg = {LO(w1 + (y & 7) * 229376), HI(w1 + (y & 7) * 229376), LO(w3 + (y & 7) * 229376), HI(w3 + (y & 7) * 229376), mb13, g < 3 ? 2048u : 1024u, ~0u, g < 3 ? ~0u : 0u};
    {
        u32x16 vi0, vi1, si;
#pragma unroll
        for (int s = 0; s < 4; ++s) { const int sr = (s + g) & 3; vi0[s] = L_X + (4 * g + (r >> 3)) * 1024 + (r & 7) * 128 + (((2 * sr + hh) ^ ((r >> 1) & 7)) << 4);
            vi0[4 + s] = L_R + (16 * sr + 8 * hh + tq) * 192 + nb * 32 + tp * 8; vi0[8 + s] = sv[s]; }
        const unsigned li = min(lane, 55), seg = y * 32 + g * 8 + li / 7; const char* pfb = y < 16 ? w1 : w3;
        vi0[12] = sel; vi0[13] = (seg & 511) * 3584 + (xcd >> 1) * 896 + (li % 7) * 128; vi0[14] = L_CX + r * 16 + g * 4; vi0[15] = L_CX + r * 16;
        const int T0 = isA ? 3 * bj : 3 * bj + 2, tile = 4 * bi + g;
        const bool vt = T0 >= 32;
        vi1[0] = hh ? L_M + 48 : L_MN + g * 128 + r * 4; vi1[1] = L_RS + g * 128 + (vt ? hh * 16 : r * 4); vi1[2] = L_RS + g * 128 + r * 4;
        const unsigned vq = lane * 16, vk = ((r & 0x13) | ((r & 4) << 1) | ((r & 8) >> 1)) * 16 + hh * 512, vv = (r >> 4) * 131072 + hh * 256 + (r & 15) * 16 + 1024;
#pragma unroll
        for (int u = 0; u < 2; ++u) { const int T = T0 + u;
            vi1[3 + 2 * u] = T < 16 ? vq : T < 32 ? vk : vv; vi1[4 + 2 * u] = vi1[3 + 2 * u] + (T < 16 ? 65536 : T < 32 ? 131072 : 512);
            const char* b = T < 16 ? ws + WS_Q + (unsigned)((T & 15) * 128 + tile) * 1024u : ws + WS_KV + (unsigned)((T & 15) * 128 + tile) * 2048u;
            si[8 + 2 * u] = LO(b); si[9 + 2 * u] = HI(b); si[12 + u] = __float_as_uint(T < 16 ? 0.25f * LOG2E : 1.f); }
        { const int T = 3 * bj + (min(lane, 5) >> 1); vi1[7] = WS_FQ + (((T >> 4) * 32 + (T & 15) * 2 + (lane & 1)) * 16 + bi) * 128; }
        vi1[8] = v1; vi1[9] = v3; vi1[10] = L_QC; vi1[11] = WS_ST + (128 * bi + 32 * g + r) * 16; vi1[12] = 1; vi1[13] = 0; vi1[14] = 0; vi1[15] = 0;
        si[0] = LO(sb); si[1] = HI(sb); si[2] = rot; si[3] = m0s; si[4] = LO(pfb); si[5] = HI(pfb); si[6] = LO(ws); si[7] = HI(ws); si[14] = L_DUM; si[15] = g == 0 ? L_DBG + (w >> 2) * 160 + 44 : L_DUM;
#pragma unroll
        for (int e = 0; e < 16; ++e) si[e] = __builtin_amdgcn_readfirstlane(si[e]);
#pragma unroll
        for (int e = 0; e < 8; ++e) sg[e] = __builtin_amdgcn_readfirstlane(sg[e]);
        asm volatile("" : "+v"(vi0), "+v"(vi1));
        { const int r1 = (rot + 1) & 7;
          if (isA) { GLDS(m0s + 16384, sv[0], sb + r1 * 196608, 0); GLDS(m0s + 20480, sv[1], sb + r1 * 196608, 0); GLDS(m0s + 24576, sv[2], sb + r1 * 196608, 0); }
          else { GLDS(m0s + r1 * 16384, sv[0], sb + r1 * 128, 0); GLDS(m0s + r1 * 16384 + 1024, sv[1], sb + r1 * 128, 0); GLDS(m0s + r1 * 16384 + 2048, sv[2], sb + r1 * 128, 0); GLDS(m0s + r1 * 16384 + 3072, sv[3], sb + r1 * 128, 0); } }
        LST(2);
        asm volatile("s_waitcnt vmcnt(6)" : "+v"(xrow), "+v"(epv) :: "memory");
        ep = (((u64)(unsigned)__builtin_amdgcn_readfirstlane(epv[1]) << 32) | (unsigned)__builtin_amdgcn_readfirstlane(epv[0])) + 1ull;
        {
            const float x0 = __uint_as_float(__builtin_amdgcn_readfirstlane(xrow[0]) << 16); float s1 = 0.f, s2 = 0.f;
#pragma unroll
            for (int e = 0; e < 4; ++e) { const float a = __uint_as_float(xrow[e] << 16) - x0, b = __uint_as_float(xrow[e] & 0xffff0000u) - x0; s1 += a + b; s2 = __builtin_fmaf(a, a, __builtin_fmaf(b, b, s2)); }
            s1 = wave_sum(s1) * (1.f / 512.f); s2 = wave_sum(s2) * (1.f / 512.f);
            const float mean = x0 + s1, den = rb(__builtin_amdgcn_sqrtf(rb(rb(__builtin_fmaxf(s2 - s1 * s1, 0.f)) + 1e-5f)));
            const u32x4 gr = {__float_as_uint(den), pk(-mean, -mean), (unsigned)ep, (unsigned)(ep >> 32)}; const unsigned go = WS_ST + (128 * bi + 8 * bj + w) * 16;
            asm volatile("s_mov_b64 exec, 1\n\tglobal_store_dwordx4 %0, %1, %2 sc1\n\ts_mov_b64 exec, -1\n\ts_nop 0" :: "v"(go), "v"(gr), "s"(ws) : "memory");
        }
        if (isA) {
            if (vt) asm volatile(
    "v_mov_b64 v[16:17], 0\n"
    "v_mov_b64 v[18:19], 0\n"
    "v_mov_b64 v[20:21], 0\n"
    "v_mov_b64 v[22:23], 0\n"
    "v_mov_b64 v[24:25], 0\n"
    "v_mov_b64 v[26:27], 0\n"
    "v_mov_b64 v[28:29], 0\n"
    "v_mov_b64 v[30:31], 0\n"
    "v_mov_b64 v[32:33], 0\n"
    "v_mov_b64 v[34:35], 0\n"
    "v_mov_b64 v[36:37], 0\n"
    "v_mov_b64 v[38:39], 0\n"
    "v_mov_b64 v[40:41], 0\n"
    "v_mov_b64 v[42:43], 0\n"
    "v_mov_b64 v[44:45], 0\n"
    "v_mov_b64 v[46:47], 0\n"
    "v_mov_b64 v[56:57], 0\n"
    "v_mov_b64 v[58:59], 0\n"
    "v_mov_b64 v[60:61], 0\n"
    "v_mov_b64 v[62:63], 0\n"
    "v_mov_b64 v[80:81], 0\n"
    "v_mov_b64 v[82:83], 0\n"
    "v_mov_b64 v[84:85], 0\n"
    "v_mov_b64 v[86:87], 0\n"
    "v_mov_b64 v[88:89], 0\n"
    "v_mov_b64 v[90:91], 0\n"
    "v_mov_b64 v[92:93], 0\n"
    "v_mov_b64 v[94:95], 0\n"
    "v_mov_b64 v[96:97], 0\n"
    "v_mov_b64 v[98:99], 0\n"
    "v_mov_b64 v[100:101], 0\n"
    "v_mov_b64 v[102:103], 0\n"
    "v_mov_b32 v104, v140\n"
    "v_mov_b32 v105, v140\n"
    "v_mov_b32 v106, v140\n"
    "v_mov_b32 v107, v140\n"
    "s_mov_b32 s56, 0\n"
    "s_lshl_b32 s58, s66, 14\n"
    "s_add_u32 s57, s66, 1\n"
    "s_add_u32 s57, s57, 1\n"
    "s_and_b32 s57, s57, 7\n"
    "s_mul_i32 s59, s57, 196608\n"
    "s_add_u32 s50, s64, s59\n"
    "s_addc_u32 s51, s65, 0\n"
    "v_add_u32 v108, s58, v128\n"
    "v_add_u32 v109, s58, v129\n"
    "v_add_u32 v110, s58, v130\n"
    "v_add_u32 v111, s58, v131\n"
    "global_load_dwordx4 v[160:163], v136, s[50:51]\n"
    "global_load_dwordx4 v[164:167], v137, s[50:51]\n"
    "global_load_dwordx4 v[168:171], v138, s[50:51]\n"
    "s_add_u32 s57, s57, 1\n"
    "s_and_b32 s57, s57, 7\n"
    "s_mul_i32 s59, s57, 196608\n"
    "s_add_u32 s50, s64, s59\n"
    "s_addc_u32 s51, s65, 0\n"
    "global_load_dwordx4 v[172:175], v136, s[50:51]\n"
    "global_load_dwordx4 v[176:179], v137, s[50:51]\n"
    "global_load_dwordx4 v[180:183], v138, s[50:51]\n"
    "s_add_u32 s57, s57, 1\n"
    "s_and_b32 s57, s57, 7\n"
    "s_mul_i32 s59, s57, 196608\n"
    "s_add_u32 s50, s64, s59\n"
    "s_addc_u32 s51, s65, 0\n"
    "s_waitcnt vmcnt(10) lgkmcnt(0)\n"
    "s_barrier\n"
    "ds_read_b128 v[48:51], v108\n"
    "ds_read_b64_tr_b16 v[64:65], v132 offset:0\n"
    "ds_read_b64_tr_b16 v[66:67], v132 offset:768\n"
    "ds_read_b64_tr_b16 v[68:69], v132 offset:64\n"
    "ds_read_b64_tr_b16 v[70:71], v132 offset:832\n"
    "ds_read_b128 v[52:55], v109\n"
    "ds_read_b64_tr_b16 v[72:73], v133 offset:0\n"
    "ds_read_b64_tr_b16 v[74:75], v133 offset:768\n"
    "ds_read_b64_tr_b16 v[76:77], v133 offset:64\n"
    "ds_read_b64_tr_b16 v[78:79], v133 offset:832\n"
    "v_mfma_f32_32x32x16_bf16 v[16:31], v[56:59], v[80:83], v[16:31]\n"
    "v_mfma_f32_32x32x16_bf16 v[32:47], v[56:59], v[84:87], v[32:47]\n"
    "ds_read_b128 v[56:59], v110\n"
    "ds_read_b64_tr_b16 v[80:81], v134 offset:0\n"
    "ds_read_b64_tr_b16 v[82:83], v134 offset:768\n"
    "ds_read_b64_tr_b16 v[84:85], v134 offset:64\n"
    "ds_read_b64_tr_b16 v[86:87], v134 offset:832\n"
    "v_mfma_f32_32x32x16_bf16 v[16:31], v[60:63], v[88:91], v[16:31]\n"
    "v_mfma_f32_32x32x16_bf16 v[32:47], v[60:63], v[92:95], v[32:47]\n"
    "ds_read_b128 v[60:63], v111\n"
    "ds_read_b64_tr_b16 v[88:89], v135 offset:0\n"
    "ds_read_b64_tr_b16 v[90:91], v135 offset:768\n"
    "ds_read_b64_tr_b16 v[92:93], v135 offset:64\n"
    "ds_read_b64_tr_b16 v[94:95], v135 offset:832\n"
    "s_waitcnt lgkmcnt(10)\n"
    "v_mfma_f32_32x32x16_bf16 v[16:31], v[48:51], v[64:67], v[16:31]\n"
    "v_mfma_f32_16x16x32_bf16 v[96:99], v[104:107], v[64:67], v[96:99]\n"
    "v_mfma_f32_32x32x16_bf16 v[32:47], v[48:51], v[68:71], v[32:47]\n"
    "v_mfma_f32_16x16x32_bf16 v[100:103], v[104:107], v[68:71], v[100:103]\n"
    "v_mfma_f32_32x32x16_bf16 v[16:31], v[52:55], v[72:75], v[16:31]\n"
    "s_add_u32 s58, s58, 0x4000\n"
    "s_and_b32 s58, s58, 0x1ffff\n"
    "v_add_u32 v108, s58, v128\n"
    "v_add_u32 v109, s58, v129\n"
    "v_add_u32 v110, s58, v130\n"
    "v_add_u32 v111, s58, v131\n"
    "v_mfma_f32_32x32x16_bf16 v[32:47], v[52:55], v[76:79], v[32:47]\n"
    "s_waitcnt vmcnt(7) lgkmcnt(0)\n"
    "s_barrier\n"
    "1:\n"
    "ds_read_b128 v[48:51], v108\n"
    "ds_read_b64_tr_b16 v[64:65], v132 offset:16384\n"
    "ds_read_b64_tr_b16 v[66:67], v132 offset:17152\n"
    "ds_read_b64_tr_b16 v[68:69], v132 offset:16448\n"
    "ds_read_b64_tr_b16 v[70:71], v132 offset:17216\n"
    "ds_read_b128 v[52:55], v109\n"
    "ds_read_b64_tr_b16 v[72:73], v133 offset:16384\n"
    "ds_read_b64_tr_b16 v[74:75], v133 offset:17152\n"
    "ds_read_b64_tr_b16 v[76:77], v133 offset:16448\n"
    "ds_read_b64_tr_b16 v[78:79], v133 offset:17216\n"
    "v_mfma_f32_32x32x16_bf16 v[16:31], v[56:59], v[80:83], v[16:31]\n"
    "v_mfma_f32_32x32x16_bf16 v[32:47], v[56:59], v[84:87], v[32:47]\n"
    "ds_read_b128 v[56:59], v110\n"
    "ds_read_b64_tr_b16 v[80:81], v134 offset:16384\n"
    "ds_read_b64_tr_b16 v[82:83], v134 offset:17152\n"
    "ds_read_b64_tr_b16 v[84:85], v134 offset:16448\n"
    "ds_read_b64_tr_b16 v[86:87], v134 offset:17216\n"
    "v_mfma_f32_32x32x16_bf16 v[16:31], v[60:63], v[88:91], v[16:31]\n"
    "v_mfma_f32_32x32x16_bf16 v[32:47], v[60:63], v[92:95], v[32:47]\n"
    "ds_read_b128 v[60:63], v111\n"
    "ds_read_b64_tr_b16 v[88:89], v135 offset:16384\n"
    "ds_read_b64_tr_b16 v[90:91], v135 offset:17152\n"
    "ds_read_b64_tr_b16 v[92:93], v135 offset:16448\n"
    "ds_read_b64_tr_b16 v[94:95], v135 offset:17216\n"
    "s_waitcnt lgkmcnt(10)\n"
    "v_mfma_f32_32x32x16_bf16 v[16:31], v[48:51], v[64:67], v[16:31]\n"
    "s_waitcnt vmcnt(3)\n"
    "ds_write_b128 v139, v[160:163] offset:0\n"
    "ds_write_b128 v139, v[164:167] offset:4096\n"
    "ds_write_b128 v139, v[168:171] offset:8192\n"
    "v_mfma_f32_16x16x32_bf16 v[96:99], v[104:107], v[64:67], v[96:99]\n"
    "global_load_dwordx4 v[160:163], v136, s[50:51]\n"
    "v_mfma_f32_32x32x16_bf16 v[32:47], v[48:51], v[68:71], v[32:47]\n"
    "global_load_dwordx4 v[164:167], v137, s[50:51]\n"
    "v_mfma_f32_16x16x32_bf16 v[100:103], v[104:107], v[68:71], v[100:103]\n"
    "v_mfma_f32_32x32x16_bf16 v[16:31], v[52:55], v[72:75], v[16:31]\n"
    "global_load_dwordx4 v[168:171], v138, s[50:51]\n"
    "s_add_u32 s58, s58, 0x4000\n"
    "s_and_b32 s58, s58, 0x1ffff\n"
    "v_add_u32 v108, s58, v128\n"
    "v_add_u32 v109, s58, v129\n"
    "v_add_u32 v110, s58, v130\n"
    "v_add_u32 v111, s58, v131\n"
    "s_add_u32 s57, s57, 1\n"
    "s_and_b32 s57, s57, 7\n"
    "s_mul_i32 s59, s57, 196608\n"
    "s_add_u32 s50, s64, s59\n"
    "s_addc_u32 s51, s65, 0\n"
    "v_mfma_f32_32x32x16_bf16 v[32:47], v[52:55], v[76:79], v[32:47]\n"
    "s_waitcnt lgkmcnt(0)\n"
    "s_barrier\n"
    "ds_read_b128 v[48:51], v108\n"
    "ds_read_b64_tr_b16 v[64:65], v132 offset:0\n"
    "ds_read_b64_tr_b16 v[66:67], v132 offset:768\n"
    "ds_read_b64_tr_b16 v[68:69], v132 offset:64\n"
    "ds_read_b64_tr_b16 v[70:71], v132 offset:832\n"
    "ds_read_b128 v[52:55], v109\n"
    "ds_read_b64_tr_b16 v[72:73], v133 offset:0\n"
    "ds_read_b64_tr_b16 v[74:75], v133 offset:768\n"
    "ds_read_b64_tr_b16 v[76:77], v133 offset:64\n"
    "ds_read_b64_tr_b16 v[78:79], v133 offset:832\n"
    "v_mfma_f32_32x32x16_bf16 v[16:31], v[56:59], v[80:83], v[16:31]\n"
    "v_mfma_f32_32x32x16_bf16 v[32:47], v[56:59], v[84:87], v[32:47]\n"
    "ds_read_b128 v[56:59], v110\n"
    "ds_read_b64_tr_b16 v[80:81], v134 offset:0\n"
    "ds_read_b64_tr_b16 v[82:83], v134 offset:768\n"
    "ds_read_b64_tr_b16 v[84:85], v134 offset:64\n"
    "ds_read_b64_tr_b16 v[86:87], v134 offset:832\n"
    "v_mfma_f32_32x32x16_bf16 v[16:31], v[60:63], v[88:91], v[16:31]\n"
    "v_mfma_f32_32x32x16_bf16 v[32:47], v[60:63], v[92:95], v[32:47]\n"
    "ds_read_b128 v[60:63], v111\n"
    "ds_read_b64_tr_b16 v[88:89], v135 offset:0\n"
    "ds_read_b64_tr_b16 v[90:91], v135 offset:768\n"
    "ds_read_b64_tr_b16 v[92:93], v135 offset:64\n"
    "ds_read_b64_tr_b16 v[94:95], v135 offset:832\n"
    "s_waitcnt lgkmcnt(10)\n"
    "v_mfma_f32_32x32x16_bf16 v[16:31], v[48:51], v[64:67], v[16:31]\n"
    "s_waitcnt vmcnt(3)\n"
    "ds_write_b128 v139, v[172:175] offset:16384\n"
    "ds_write_b128 v139, v[176:179] offset:20480\n"
    "ds_write_b128 v139, v[180:183] offset:24576\n"
    "v_mfma_f32_16x16x32_bf16 v[96:99], v[104:107], v[64:67], v[96:99]\n"
    "global_load_dwordx4 v[172:175], v136, s[50:51]\n"
    "v_mfma_f32_32x32x16_bf16 v[32:47], v[48:51], v[68:71], v[32:47]\n"
    "global_load_dwordx4 v[176:179], v137, s[50:51]\n"
    "v_mfma_f32_16x16x32_bf16 v[100:103], v[104:107], v[68:71], v[100:103]\n"
    "v_mfma_f32_32x32x16_bf16 v[16:31], v[52:55], v[72:75], v[16:31]\n"
    "global_load_dwordx4 v[180:183], v138, s[50:51]\n"
    "s_add_u32 s58, s58, 0x4000\n"
    "s_and_b32 s58, s58, 0x1ffff\n"
    "v_add_u32 v108, s58, v128\n"
    "v_add_u32 v109, s58, v129\n"
    "v_add_u32 v110, s58, v130\n"
    "v_add_u32 v111, s58, v131\n"
    "s_add_u32 s57, s57, 1\n"
    "s_and_b32 s57, s57, 7\n"
    "s_mul_i32 s59, s57, 196608\n"
    "s_add_u32 s50, s64, s59\n"
    "s_addc_u32 s51, s65, 0\n"
    "v_mfma_f32_32x32x16_bf16 v[32:47], v[52:55], v[76:79], v[32:47]\n"
    "s_add_u32 s56, s56, 1\n"
    "s_cmp_lt_u32 s56, 2\n"
    "s_waitcnt lgkmcnt(0)\n"
    "s_barrier\n"
    "s_cbranch_scc1 1b\n"
    "ds_read_b128 v[48:51], v108\n"
    "ds_read_b64_tr_b16 v[64:65], v132 offset:16384\n"
    "ds_read_b64_tr_b16 v[66:67], v132 offset:17152\n"
    "ds_read_b64_tr_b16 v[68:69], v132 offset:16448\n"
    "ds_read_b64_tr_b16 v[70:71], v132 offset:17216\n"
    "ds_read_b128 v[52:55], v109\n"
    "ds_read_b64_tr_b16 v[72:73], v133 offset:16384\n"
    "ds_read_b64_tr_b16 v[74:75], v133 offset:17152\n"
    "ds_read_b64_tr_b16 v[76:77], v133 offset:16448\n"
    "ds_read_b64_tr_b16 v[78:79], v133 offset:17216\n"
    "v_mfma_f32_32x32x16_bf16 v[16:31], v[56:59], v[80:83], v[16:31]\n"
    "v_mfma_f32_32x32x16_bf16 v[32:47], v[56:59], v[84:87], v[32:47]\n"
    "ds_read_b128 v[56:59], v110\n"
    "ds_read_b64_tr_b16 v[80:81], v134 offset:16384\n"
    "ds_read_b64_tr_b16 v[82:83], v134 offset:17152\n"
    "ds_read_b64_tr_b16 v[84:85], v134 offset:16448\n"
    "ds_read_b64_tr_b16 v[86:87], v134 offset:17216\n"
    "v_mfma_f32_32x32x16_bf16 v[16:31], v[60:63], v[88:91], v[16:31]\n"
    "v_mfma_f32_32x32x16_bf16 v[32:47], v[60:63], v[92:95], v[32:47]\n"
    "ds_read_b128 v[60:63], v111\n"
    "ds_read_b64_tr_b16 v[88:89], v135 offset:16384\n"
    "ds_read_b64_tr_b16 v[90:91], v135 offset:17152\n"
    "ds_read_b64_tr_b16 v[92:93], v135 offset:16448\n"
    "ds_read_b64_tr_b16 v[94:95], v135 offset:17216\n"
    "s_waitcnt lgkmcnt(10)\n"
    "v_mfma_f32_32x32x16_bf16 v[16:31], v[48:51], v[64:67], v[16:31]\n"
    "s_waitcnt vmcnt(3)\n"
    "ds_write_b128 v139, v[160:163] offset:0\n"
    "ds_write_b128 v139, v[164:167] offset:4096\n"
    "ds_write_b128 v139, v[168:171] offset:8192\n"
    "v_mfma_f32_16x16x32_bf16 v[96:99], v[104:107], v[64:67], v[96:99]\n"
    "v_mfma_f32_32x32x16_bf16 v[32:47], v[48:51], v[68:71], v[32:47]\n"
    "v_mfma_f32_16x16x32_bf16 v[100:103], v[104:107], v[68:71], v[100:103]\n"
    "v_mfma_f32_32x32x16_bf16 v[16:31], v[52:55], v[72:75], v[16:31]\n"
    "s_add_u32 s58, s58, 0x4000\n"
    "s_and_b32 s58, s58, 0x1ffff\n"
    "v_add_u32 v108, s58, v128\n"
    "v_add_u32 v109, s58, v129\n"
    "v_add_u32 v110, s58, v130\n"
    "v_add_u32 v111, s58, v131\n"
    "v_mfma_f32_32x32x16_bf16 v[32:47], v[52:55], v[76:79], v[32:47]\n"
    "s_waitcnt lgkmcnt(0)\n"
    "s_barrier\n"
    "ds_read_b128 v[48:51], v108\n"
    "ds_read_b64_tr_b16 v[64:65], v132 offset:0\n"
    "ds_read_b64_tr_b16 v[66:67], v132 offset:768\n"
    "ds_read_b64_tr_b16 v[68:69], v132 offset:64\n"
    "ds_read_b64_tr_b16 v[70:71], v132 offset:832\n"
    "ds_read_b128 v[52:55], v109\n"
    "ds_read_b64_tr_b16 v[72:73], v133 offset:0\n"
    "ds_read_b64_tr_b16 v[74:75], v133 offset:768\n"
    "ds_read_b64_tr_b16 v[76:77], v133 offset:64\n"
    "ds_read_b64_tr_b16 v[78:79], v133 offset:832\n"
    "v_mfma_f32_32x32x16_bf16 v[16:31], v[56:59], v[80:83], v[16:31]\n"
    "v_mfma_f32_32x32x16_bf16 v[32:47], v[56:59], v[84:87], v[32:47]\n"
    "ds_read_b128 v[56:59], v110\n"
    "ds_read_b64_tr_b16 v[80:81], v134 offset:0\n"
    "ds_read_b64_tr_b16 v[82:83], v134 offset:768\n"
    "ds_read_b64_tr_b16 v[84:85], v134 offset:64\n"
    "ds_read_b64_tr_b16 v[86:87], v134 offset:832\n"
    "v_mfma_f32_32x32x16_bf16 v[16:31], v[60:63], v[88:91], v[16:31]\n"
    "v_mfma_f32_32x32x16_bf16 v[32:47], v[60:63], v[92:95], v[32:47]\n"
    "ds_read_b128 v[60:63], v111\n"
    "ds_read_b64_tr_b16 v[88:89], v135 offset:0\n"
    "ds_read_b64_tr_b16 v[90:91], v135 offset:768\n"
    "ds_read_b64_tr_b16 v[92:93], v135 offset:64\n"
    "ds_read_b64_tr_b16 v[94:95], v135 offset:832\n"
    "s_waitcnt lgkmcnt(10)\n"
    "v_mfma_f32_32x32x16_bf16 v[16:31], v[48:51], v[64:67], v[16:31]\n"
    "s_waitcnt vmcnt(0)\n"
    "ds_write_b128 v139, v[172:175] offset:16384\n"
    "ds_write_b128 v139, v[176:179] offset:20480\n"
    "ds_write_b128 v139, v[180:183] offset:24576\n"
    "v_mfma_f32_16x16x32_bf16 v[96:99], v[104:107], v[64:67], v[96:99]\n"
    "v_mfma_f32_32x32x16_bf16 v[32:47], v[48:51], v[68:71], v[32:47]\n"
    "v_mfma_f32_16x16x32_bf16 v[100:103], v[104:107], v[68:71], v[100:103]\n"
    "v_mfma_f32_32x32x16_bf16 v[16:31], v[52:55], v[72:75], v[16:31]\n"
    "s_add_u32 s58, s58, 0x4000\n"
    "s_and_b32 s58, s58, 0x1ffff\n"
    "v_add_u32 v108, s58, v128\n"
    "v_add_u32 v109, s58, v129\n"
    "v_add_u32 v110, s58, v130\n"
    "v_add_u32 v111, s58, v131\n"
    "v_mfma_f32_32x32x16_bf16 v[32:47], v[52:55], v[76:79], v[32:47]\n"
    "s_waitcnt lgkmcnt(0)\n"
    "s_barrier\n"
    "v_mfma_f32_32x32x16_bf16 v[16:31], v[56:59], v[80:83], v[16:31]\n"
    "ds_read_b128 v[48:51], v108\n"
    "ds_read_b64_tr_b16 v[64:65], v132 offset:16384\n"
    "ds_read_b64_tr_b16 v[66:67], v132 offset:17152\n"
    "ds_read_b64_tr_b16 v[68:69], v132 offset:16448\n"
    "ds_read_b64_tr_b16 v[70:71], v132 offset:17216\n"
    "v_mfma_f32_32x32x16_bf16 v[32:47], v[56:59], v[84:87], v[32:47]\n"
    "ds_read_b128 v[52:55], v109\n"
    "ds_read_b64_tr_b16 v[72:73], v133 offset:16384\n"
    "ds_read_b64_tr_b16 v[74:75], v133 offset:17152\n"
    "ds_read_b64_tr_b16 v[76:77], v133 offset:16448\n"
    "ds_read_b64_tr_b16 v[78:79], v133 offset:17216\n"
    "v_mfma_f32_32x32x16_bf16 v[16:31], v[60:63], v[88:91], v[16:31]\n"
    "v_mfma_f32_32x32x16_bf16 v[32:47], v[60:63], v[92:95], v[32:47]\n"
    "ds_read_b128 v[56:59], v110\n"
    "ds_read_b64_tr_b16 v[80:81], v134 offset:16384\n"
    "ds_read_b64_tr_b16 v[82:83], v134 offset:17152\n"
    "ds_read_b64_tr_b16 v[84:85], v134 offset:16448\n"
    "ds_read_b64_tr_b16 v[86:87], v134 offset:17216\n"
    "s_waitcnt lgkmcnt(10)\n"
    "v_mfma_f32_32x32x16_bf16 v[16:31], v[48:51], v[64:67], v[16:31]\n"
    "v_mfma_f32_16x16x32_bf16 v[96:99], v[104:107], v[64:67], v[96:99]\n"
    "ds_read_b128 v[60:63], v111\n"
    "ds_read_b64_tr_b16 v[88:89], v135 offset:16384\n"
    "ds_read_b64_tr_b16 v[90:91], v135 offset:17152\n"
    "ds_read_b64_tr_b16 v[92:93], v135 offset:16448\n"
    "ds_read_b64_tr_b16 v[94:95], v135 offset:17216\n"
    "v_mfma_f32_32x32x16_bf16 v[32:47], v[48:51], v[68:71], v[32:47]\n"
    "v_mfma_f32_16x16x32_bf16 v[100:103], v[104:107], v[68:71], v[100:103]\n"
    "s_waitcnt lgkmcnt(10)\n"
    "v_mfma_f32_32x32x16_bf16 v[16:31], v[52:55], v[72:75], v[16:31]\n"
    "v_mfma_f32_32x32x16_bf16 v[32:47], v[52:55], v[76:79], v[32:47]\n"
    "s_waitcnt lgkmcnt(0)\n"
    "v_mfma_f32_32x32x16_bf16 v[16:31], v[56:59], v[80:83], v[16:31]\n"
    "s_nop 3\n"
    "v_permlane16_swap_b32 v96, v97\n"
    "v_cvt_pk_bf16_f32 v114, v96, v96\n"
    "v_lshlrev_b32 v114, 16, v114\n"
    "v_sub_f32 v114, v96, v114\n"
    "v_cvt_pk_bf16_f32 v114, v96, v114\n"
    "v_mfma_f32_32x32x16_bf16 v[32:47], v[56:59], v[84:87], v[32:47]\n"
    "v_permlane16_swap_b32 v100, v101\n"
    "v_cvt_pk_bf16_f32 v115, v100, v100\n"
    "v_lshlrev_b32 v115, 16, v115\n"
    "v_sub_f32 v115, v100, v115\n"
    "v_cvt_pk_bf16_f32 v115, v100, v115\n"
    "v_mfma_f32_32x32x16_bf16 v[16:31], v[60:63], v[88:91], v[16:31]\n"
    "s_mov_b64 exec, 0xffffffff\n"
    "ds_write_b32 v142, v114\n"
    "ds_write_b32 v142, v115 offset:512\n"
    "s_mov_b64 exec, -1\n"
    "v_mfma_f32_32x32x16_bf16 v[32:47], v[60:63], v[92:95], v[32:47]\n"
    "s_waitcnt lgkmcnt(0)\n"
    "s_barrier\n"
    "ds_read_b32 v48, v144\n"
    "ds_read_b32 v49, v144\n"
    "ds_read_b32 v50, v144\n"
    "ds_read_b32 v51, v144\n"
    "ds_read_b128 v[52:55], v143 offset:0\n"
    "ds_read_b128 v[56:59], v143 offset:512\n"
    "ds_read_b128 v[64:67], v145 offset:0\n"
    "ds_read_b128 v[68:71], v145 offset:32\n"
    "ds_read_b128 v[72:75], v145 offset:64\n"
    "ds_read_b128 v[76:79], v145 offset:96\n"
    "s_waitcnt lgkmcnt(4)\n"
    "v_mfma_f32_32x32x16_bf16 v[16:31], v[48:51], v[52:55], v[16:31]\n"
    "v_mfma_f32_32x32x16_bf16 v[32:47], v[48:51], v[56:59], v[32:47]\n"
    "s_waitcnt lgkmcnt(0)\n"
    "s_nop 10\n"
    "v_pk_mul_f32 v[16:17], v[16:17], v[64:65]\n"
    "v_pk_mul_f32 v[18:19], v[18:19], v[66:67]\n"
    "v_pk_mul_f32 v[20:21], v[20:21], v[68:69]\n"
    "v_pk_mul_f32 v[22:23], v[22:23], v[70:71]\n"
    "v_cvt_pk_bf16_f32 v80, v16, v17\n"
    "v_cvt_pk_bf16_f32 v81, v18, v19\n"
    "v_cvt_pk_bf16_f32 v82, v20, v21\n"
    "v_cvt_pk_bf16_f32 v83, v22, v23\n"
    "global_store_dwordx4 v147, v[80:83], s[72:73] sc1\n"
    "v_pk_mul_f32 v[24:25], v[24:25], v[72:73]\n"
    "v_pk_mul_f32 v[26:27], v[26:27], v[74:75]\n"
    "v_pk_mul_f32 v[28:29], v[28:29], v[76:77]\n"
    "v_pk_mul_f32 v[30:31], v[30:31], v[78:79]\n"
    "v_cvt_pk_bf16_f32 v84, v24, v25\n"
    "v_cvt_pk_bf16_f32 v85, v26, v27\n"
    "v_cvt_pk_bf16_f32 v86, v28, v29\n"
    "v_cvt_pk_bf16_f32 v87, v30, v31\n"
    "global_store_dwordx4 v148, v[84:87], s[72:73] sc1\n"
    "v_pk_mul_f32 v[32:33], v[32:33], v[64:65]\n"
    "v_pk_mul_f32 v[34:35], v[34:35], v[66:67]\n"
    "v_pk_mul_f32 v[36:37], v[36:37], v[68:69]\n"
    "v_pk_mul_f32 v[38:39], v[38:39], v[70:71]\n"
    "v_cvt_pk_bf16_f32 v88, v32, v33\n"
    "v_cvt_pk_bf16_f32 v89, v34, v35\n"
    "v_cvt_pk_bf16_f32 v90, v36, v37\n"
    "v_cvt_pk_bf16_f32 v91, v38, v39\n"
    "global_store_dwordx4 v149, v[88:91], s[74:75] sc1\n"
    "v_pk_mul_f32 v[40:41], v[40:41], v[72:73]\n"
    "v_pk_mul_f32 v[42:43], v[42:43], v[74:75]\n"
    "v_pk_mul_f32 v[44:45], v[44:45], v[76:77]\n"
    "v_pk_mul_f32 v[46:47], v[46:47], v[78:79]\n"
    "v_cvt_pk_bf16_f32 v92, v40, v41\n"
    "v_cvt_pk_bf16_f32 v93, v42, v43\n"
    "v_cvt_pk_bf16_f32 v94, v44, v45\n"
    "v_cvt_pk_bf16_f32 v95, v46, v47\n"
    "global_store_dwordx4 v150, v[92:95], s[74:75] sc1\n"
    "v_mov_b32 v114, s88\n"
    "v_mov_b32 v115, s89\n"
    "s_mov_b64 exec, 1\n"
    "s_waitcnt vmcnt(0)\n"
    "ds_add_rtn_u32 v116, v154, v156\n"
    "s_waitcnt lgkmcnt(0)\n"
    "v_readfirstlane_b32 s59, v116\n"
    "s_mov_b64 exec, 0x3f\n"
    "s_cmp_eq_u32 s59, 7\n"
    "s_cbranch_scc0 2f\n"
    "global_store_dwordx2 v151, v[114:115], s[70:71] sc1\n"
    "2:\n"
    "s_mov_b64 exec, -1\n"
                P1_OPERANDS);
            else asm volatile(
    "v_mov_b64 v[16:17], 0\n"
    "v_mov_b64 v[18:19], 0\n"
    "v_mov_b64 v[20:21], 0\n"
    "v_mov_b64 v[22:23], 0\n"
    "v_mov_b64 v[24:25], 0\n"
    "v_mov_b64 v[26:27], 0\n"
    "v_mov_b64 v[28:29], 0\n"
    "v_mov_b64 v[30:31], 0\n"
    "v_mov_b64 v[32:33], 0\n"
    "v_mov_b64 v[34:35], 0\n"
    "v_mov_b64 v[36:37], 0\n"
    "v_mov_b64 v[38:39], 0\n"
    "v_mov_b64 v[40:41], 0\n"
    "v_mov_b64 v[42:43], 0\n"
    "v_mov_b64 v[44:45], 0\n"
    "v_mov_b64 v[46:47], 0\n"
    "v_mov_b64 v[56:57], 0\n"
    "v_mov_b64 v[58:59], 0\n"
    "v_mov_b64 v[60:61], 0\n"
    "v_mov_b64 v[62:63], 0\n"
    "v_mov_b64 v[80:81], 0\n"
    "v_mov_b64 v[82:83], 0\n"
    "v_mov_b64 v[84:85], 0\n"
    "v_mov_b64 v[86:87], 0\n"
    "v_mov_b64 v[88:89], 0\n"
    "v_mov_b64 v[90:91], 0\n"
    "v_mov_b64 v[92:93], 0\n"
    "v_mov_b64 v[94:95], 0\n"
    "v_mov_b64 v[96:97], 0\n"
    "v_mov_b64 v[98:99], 0\n"
    "v_mov_b64 v[100:101], 0\n"
    "v_mov_b64 v[102:103], 0\n"
    "v_mov_b32 v104, v140\n"
    "v_mov_b32 v105, v140\n"
    "v_mov_b32 v106, v140\n"
    "v_mov_b32 v107, v140\n"
    "s_mov_b32 s56, 0\n"
    "s_lshl_b32 s58, s66, 14\n"
    "s_add_u32 s57, s66, 1\n"
    "s_add_u32 s57, s57, 1\n"
    "s_and_b32 s57, s57, 7\n"
    "s_mul_i32 s59, s57, 196608\n"
    "s_add_u32 s50, s64, s59\n"
    "s_addc_u32 s51, s65, 0\n"
    "v_add_u32 v108, s58, v128\n"
    "v_add_u32 v109, s58, v129\n"
    "v_add_u32 v110, s58, v130\n"
    "v_add_u32 v111, s58, v131\n"
    "global_load_dwordx4 v[160:163], v136, s[50:51]\n"
    "global_load_dwordx4 v[164:167], v137, s[50:51]\n"
    "global_load_dwordx4 v[168:171], v138, s[50:51]\n"
    "s_add_u32 s57, s57, 1\n"
    "s_and_b32 s57, s57, 7\n"
    "s_mul_i32 s59, s57, 196608\n"
    "s_add_u32 s50, s64, s59\n"
    "s_addc_u32 s51, s65, 0\n"
    "global_load_dwordx4 v[172:175], v136, s[50:51]\n"
    "global_load_dwordx4 v[176:179], v137, s[50:51]\n"
    "global_load_dwordx4 v[180:183], v138, s[50:51]\n"
    "s_add_u32 s57, s57, 1\n"
    "s_and_b32 s57, s57, 7\n"
    "s_mul_i32 s59, s57, 196608\n"
    "s_add_u32 s50, s64, s59\n"
    "s_addc_u32 s51, s65, 0\n"
    "s_waitcnt vmcnt(10) lgkmcnt(0)\n"
    "s_barrier\n"
    "ds_read_b128 v[48:51], v108\n"
    "ds_read_b64_tr_b16 v[64:65], v132 offset:0\n"
    "ds_read_b64_tr_b16 v[66:67], v132 offset:768\n"
    "ds_read_b64_tr_b16 v[68:69], v132 offset:64\n"
    "ds_read_b64_tr_b16 v[70:71], v132 offset:832\n"
    "ds_read_b128 v[52:55], v109\n"
    "ds_read_b64_tr_b16 v[72:73], v133 offset:0\n"
    "ds_read_b64_tr_b16 v[74:75], v133 offset:768\n"
    "ds_read_b64_tr_b16 v[76:77], v133 offset:64\n"
    "ds_read_b64_tr_b16 v[78:79], v133 offset:832\n"
    "v_mfma_f32_32x32x16_bf16 v[16:31], v[80:83], v[56:59], v[16:31]\n"
    "v_mfma_f32_32x32x16_bf16 v[32:47], v[84:87], v[56:59], v[32:47]\n"
    "ds_read_b128 v[56:59], v110\n"
    "ds_read_b64_tr_b16 v[80:81], v134 offset:0\n"
    "ds_read_b64_tr_b16 v[82:83], v134 offset:768\n"
    "ds_read_b64_tr_b16 v[84:85], v134 offset:64\n"
    "ds_read_b64_tr_b16 v[86:87], v134 offset:832\n"
    "v_mfma_f32_32x32x16_bf16 v[16:31], v[88:91], v[60:63], v[16:31]\n"
    "v_mfma_f32_32x32x16_bf16 v[32:47], v[92:95], v[60:63], v[32:47]\n"
    "ds_read_b128 v[60:63], v111\n"
    "ds_read_b64_tr_b16 v[88:89], v135 offset:0\n"
    "ds_read_b64_tr_b16 v[90:91], v135 offset:768\n"
    "ds_read_b64_tr_b16 v[92:93], v135 offset:64\n"
    "ds_read_b64_tr_b16 v[94:95], v135 offset:832\n"
    "s_waitcnt lgkmcnt(10)\n"
    "v_mfma_f32_32x32x16_bf16 v[16:31], v[64:67], v[48:51], v[16:31]\n"
    "v_mfma_f32_16x16x32_bf16 v[96:99], v[104:107], v[64:67], v[96:99]\n"
    "v_mfma_f32_32x32x16_bf16 v[32:47], v[68:71], v[48:51], v[32:47]\n"
    "v_mfma_f32_16x16x32_bf16 v[100:103], v[104:107], v[68:71], v[100:103]\n"
    "v_mfma_f32_32x32x16_bf16 v[16:31], v[72:75], v[52:55], v[16:31]\n"
    "s_add_u32 s58, s58, 0x4000\n"
    "s_and_b32 s58, s58, 0x1ffff\n"
    "v_add_u32 v108, s58, v128\n"
    "v_add_u32 v109, s58, v129\n"
    "v_add_u32 v110, s58, v130\n"
    "v_add_u32 v111, s58, v131\n"
    "v_mfma_f32_32x32x16_bf16 v[32:47], v[76:79], v[52:55], v[32:47]\n"
    "s_waitcnt vmcnt(7) lgkmcnt(0)\n"
    "s_barrier\n"
    "1:\n"
    "ds_read_b128 v[48:51], v108\n"
    "ds_read_b64_tr_b16 v[64:65], v132 offset:16384\n"
    "ds_read_b64_tr_b16 v[66:67], v132 offset:17152\n"
    "ds_read_b64_tr_b16 v[68:69], v132 offset:16448\n"
    "ds_read_b64_tr_b16 v[70:71], v132 offset:17216\n"
    "ds_read_b128 v[52:55], v109\n"
    "ds_read_b64_tr_b16 v[72:73], v133 offset:16384\n"
    "ds_read_b64_tr_b16 v[74:75], v133 offset:17152\n"
    "ds_read_b64_tr_b16 v[76:77], v133 offset:16448\n"
    "ds_read_b64_tr_b16 v[78:79], v133 offset:17216\n"
    "v_mfma_f32_32x32x16_bf16 v[16:31], v[80:83], v[56:59], v[16:31]\n"
    "v_mfma_f32_32x32x16_bf16 v[32:47], v[84:87], v[56:59], v[32:47]\n"
    "ds_read_b128 v[56:59], v110\n"
    "ds_read_b64_tr_b16 v[80:81], v134 offset:16384\n"
    "ds_read_b64_tr_b16 v[82:83], v134 offset:17152\n"
    "ds_read_b64_tr_b16 v[84:85], v134 offset:16448\n"
    "ds_read_b64_tr_b16 v[86:87], v134 offset:17216\n"
    "v_mfma_f32_32x32x16_bf16 v[16:31], v[88:91], v[60:63], v[16:31]\n"
    "v_mfma_f32_32x32x16_bf16 v[32:47], v[92:95], v[60:63], v[32:47]\n"
    "ds_read_b128 v[60:63], v111\n"
    "ds_read_b64_tr_b16 v[88:89], v135 offset:16384\n"
    "ds_read_b64_tr_b16 v[90:91], v135 offset:17152\n"
    "ds_read_b64_tr_b16 v[92:93], v135 offset:16448\n"
    "ds_read_b64_tr_b16 v[94:95], v135 offset:17216\n"
    "s_waitcnt lgkmcnt(10)\n"
    "v_mfma_f32_32x32x16_bf16 v[16:31], v[64:67], v[48:51], v[16:31]\n"
    "s_waitcnt vmcnt(3)\n"
    "ds_write_b128 v139, v[160:163] offset:0\n"
    "ds_write_b128 v139, v[164:167] offset:4096\n"
    "ds_write_b128 v139, v[168:171] offset:8192\n"
    "v_mfma_f32_16x16x32_bf16 v[96:99], v[104:107], v[64:67], v[96:99]\n"
    "global_load_dwordx4 v[160:163], v136, s[50:51]\n"
    "v_mfma_f32_32x32x16_bf16 v[32:47], v[68:71], v[48:51], v[32:47]\n"
    "global_load_dwordx4 v[164:167], v137, s[50:51]\n"
    "v_mfma_f32_16x16x32_bf16 v[100:103], v[104:107], v[68:71], v[100:103]\n"
    "v_mfma_f32_32x32x16_bf16 v[16:31], v[72:75], v[52:55], v[16:31]\n"
    "global_load_dwordx4 v[168:171], v138, s[50:51]\n"
    "s_add_u32 s58, s58, 0x4000\n"
    "s_and_b32 s58, s58, 0x1ffff\n"
    "v_add_u32 v108, s58, v128\n"
    "v_add_u32 v109, s58, v129\n"
    "v_add_u32 v110, s58, v130\n"
    "v_add_u32 v111, s58, v131\n"
    "s_add_u32 s57, s57, 1\n"
    "s_and_b32 s57, s57, 7\n"
    "s_mul_i32 s59, s57, 196608\n"
    "s_add_u32 s50, s64, s59\n"
    "s_addc_u32 s51, s65, 0\n"
    "v_mfma_f32_32x32x16_bf16 v[32:47], v[76:79], v[52:55], v[32:47]\n"
    "s_waitcnt lgkmcnt(0)\n"
    "s_barrier\n"
    "ds_read_b128 v[48:51], v108\n"
    "ds_read_b64_tr_b16 v[64:65], v132 offset:0\n"
    "ds_read_b64_tr_b16 v[66:67], v132 offset:768\n"
    "ds_read_b64_tr_b16 v[68:69], v132 offset:64\n"
    "ds_read_b64_tr_b16 v[70:71], v132 offset:832\n"
    "ds_read_b128 v[52:55], v109\n"
    "ds_read_b64_tr_b16 v[72:73], v133 offset:0\n"
    "ds_read_b64_tr_b16 v[74:75], v133 offset:768\n"
    "ds_read_b64_tr_b16 v[76:77], v133 offset:64\n"
    "ds_read_b64_tr_b16 v[78:79], v133 offset:832\n"
    "v_mfma_f32_32x32x16_bf16 v[16:31], v[80:83], v[56:59], v[16:31]\n"
    "v_mfma_f32_32x32x16_bf16 v[32:47], v[84:87], v[56:59], v[32:47]\n"
    "ds_read_b128 v[56:59], v110\n"
    "ds_read_b64_tr_b16 v[80:81], v134 offset:0\n"
    "ds_read_b64_tr_b16 v[82:83], v134 offset:768\n"
    "ds_read_b64_tr_b16 v[84:85], v134 offset:64\n"
    "ds_read_b64_tr_b16 v[86:87], v134 offset:832\n"
    "v_mfma_f32_32x32x16_bf16 v[16:31], v[88:91], v[60:63], v[16:31]\n"
    "v_mfma_f32_32x32x16_bf16 v[32:47], v[92:95], v[60:63], v[32:47]\n"
    "ds_read_b128 v[60:63], v111\n"
    "ds_read_b64_tr_b16 v[88:89], v135 offset:0\n"
    "ds_read_b64_tr_b16 v[90:91], v135 offset:768\n"
    "ds_read_b64_tr_b16 v[92:93], v135 offset:64\n"
    "ds_read_b64_tr_b16 v[94:95], v135 offset:832\n"
    "s_waitcnt lgkmcnt(10)\n"
    "v_mfma_f32_32x32x16_bf16 v[16:31], v[64:67], v[48:51], v[16:31]\n"
    "s_waitcnt vmcnt(3)\n"
    "ds_write_b128 v139, v[172:175] offset:16384\n"
    "ds_write_b128 v139, v[176:179] offset:20480\n"
    "ds_write_b128 v139, v[180:183] offset:24576\n"
    "v_mfma_f32_16x16x32_bf16 v[96:99], v[104:107], v[64:67], v[96:99]\n"
    "global_load_dwordx4 v[172:175], v136, s[50:51]\n"
    "v_mfma_f32_32x32x16_bf16 v[32:47], v[68:71], v[48:51], v[32:47]\n"
    "global_load_dwordx4 v[176:179], v137, s[50:51]\n"
    "v_mfma_f32_16x16x32_bf16 v[100:103], v[104:107], v[68:71], v[100:103]\n"
    "v_mfma_f32_32x32x16_bf16 v[16:31], v[72:75], v[52:55], v[16:31]\n"
    "global_load_dwordx4 v[180:183], v138, s[50:51]\n"
    "s_add_u32 s58, s58, 0x4000\n"
    "s_and_b32 s58, s58, 0x1ffff\n"
    "v_add_u32 v108, s58, v128\n"
    "v_add_u32 v109, s58, v129\n"
    "v_add_u32 v110, s58, v130\n"
    "v_add_u32 v111, s58, v131\n"
    "s_add_u32 s57, s57, 1\n"
    "s_and_b32 s57, s57, 7\n"
    "s_mul_i32 s59, s57, 196608\n"
    "s_add_u32 s50, s64, s59\n"
    "s_addc_u32 s51, s65, 0\n"
    "v_mfma_f32_32x32x16_bf16 v[32:47], v[76:79], v[52:55], v[32:47]\n"
    "s_add_u32 s56, s56, 1\n"
    "s_cmp_lt_u32 s56, 2\n"
    "s_waitcnt lgkmcnt(0)\n"
    "s_barrier\n"
    "s_cbranch_scc1 1b\n"
    "ds_read_b128 v[48:51], v108\n"
    "ds_read_b64_tr_b16 v[64:65], v132 offset:16384\n"
    "ds_read_b64_tr_b16 v[66:67], v132 offset:17152\n"
    "ds_read_b64_tr_b16 v[68:69], v132 offset:16448\n"
    "ds_read_b64_tr_b16 v[70:71], v132 offset:17216\n"
    "ds_read_b128 v[52:55], v109\n"
    "ds_read_b64_tr_b16 v[72:73], v133 offset:16384\n"
    "ds_read_b64_tr_b16 v[74:75], v133 offset:17152\n"
    "ds_read_b64_tr_b16 v[76:77], v133 offset:16448\n"
    "ds_read_b64_tr_b16 v[78:79], v133 offset:17216\n"
    "v_mfma_f32_32x32x16_bf16 v[16:31], v[80:83], v[56:59], v[16:31]\n"
    "v_mfma_f32_32x32x16_bf16 v[32:47], v[84:87], v[56:59], v[32:47]\n"
    "ds_read_b128 v[56:59], v110\n"
    "ds_read_b64_tr_b16 v[80:81], v134 offset:16384\n"
    "ds_read_b64_tr_b16 v[82:83], v134 offset:17152\n"
    "ds_read_b64_tr_b16 v[84:85], v134 offset:16448\n"
    "ds_read_b64_tr_b16 v[86:87], v134 offset:17216\n"
    "v_mfma_f32_32x32x16_bf16 v[16:31], v[88:91], v[60:63], v[16:31]\n"
    "v_mfma_f32_32x32x16_bf16 v[32:47], v[92:95], v[60:63], v[32:47]\n"
    "ds_read_b128 v[60:63], v111\n"
    "ds_read_b64_tr_b16 v[88:89], v135 offset:16384\n"
    "ds_read_b64_tr_b16 v[90:91], v135 offset:17152\n"
    "ds_read_b64_tr_b16 v[92:93], v135 offset:16448\n"
    "ds_read_b64_tr_b16 v[94:95], v135 offset:17216\n"
    "s_waitcnt lgkmcnt(10)\n"
    "v_mfma_f32_32x32x16_bf16 v[16:31], v[64:67], v[48:51], v[16:31]\n"
    "s_waitcnt vmcnt(3)\n"
    "ds_write_b128 v139, v[160:163] offset:0\n"
    "ds_write_b128 v139, v[164:167] offset:4096\n"
    "ds_write_b128 v139, v[168:171] offset:8192\n"
    "v_mfma_f32_16x16x32_bf16 v[96:99], v[104:107], v[64:67], v[96:99]\n"
    "v_mfma_f32_32x32x16_bf16 v[32:47], v[68:71], v[48:51], v[32:47]\n"
    "v_mfma_f32_16x16x32_bf16 v[100:103], v[104:107], v[68:71], v[100:103]\n"
    "v_mfma_f32_32x32x16_bf16 v[16:31], v[72:75], v[52:55], v[16:31]\n"
    "s_add_u32 s58, s58, 0x4000\n"
    "s_and_b32 s58, s58, 0x1ffff\n"
    "v_add_u32 v108, s58, v128\n"
    "v_add_u32 v109, s58, v129\n"
    "v_add_u32 v110, s58, v130\n"
    "v_add_u32 v111, s58, v131\n"
    "v_mfma_f32_32x32x16_bf16 v[32:47], v[76:79], v[52:55], v[32:47]\n"
    "s_waitcnt lgkmcnt(0)\n"
    "s_barrier\n"
    "ds_read_b128 v[48:51], v108\n"
    "ds_read_b64_tr_b16 v[64:65], v132 offset:0\n"
    "ds_read_b64_tr_b16 v[66:67], v132 offset:768\n"
    "ds_read_b64_tr_b16 v[68:69], v132 offset:64\n"
    "ds_read_b64_tr_b16 v[70:71], v132 offset:832\n"
    "ds_read_b128 v[52:55], v109\n"
    "ds_read_b64_tr_b16 v[72:73], v133 offset:0\n"
    "ds_read_b64_tr_b16 v[74:75], v133 offset:768\n"
    "ds_read_b64_tr_b16 v[76:77], v133 offset:64\n"
    "ds_read_b64_tr_b16 v[78:79], v133 offset:832\n"
    "v_mfma_f32_32x32x16_bf16 v[16:31], v[80:83], v[56:59], v[16:31]\n"
    "v_mfma_f32_32x32x16_bf16 v[32:47], v[84:87], v[56:59], v[32:47]\n"
    "ds_read_b128 v[56:59], v110\n"
    "ds_read_b64_tr_b16 v[80:81], v134 offset:0\n"
    "ds_read_b64_tr_b16 v[82:83], v134 offset:768\n"
    "ds_read_b64_tr_b16 v[84:85], v134 offset:64\n"
    "ds_read_b64_tr_b16 v[86:87], v134 offset:832\n"
    "v_mfma_f32_32x32x16_bf16 v[16:31], v[88:91], v[60:63], v[16:31]\n"
    "v_mfma_f32_32x32x16_bf16 v[32:47], v[92:95], v[60:63], v[32:47]\n"
    "ds_read_b128 v[60:63], v111\n"
    "ds_read_b64_tr_b16 v[88:89], v135 offset:0\n"
    "ds_read_b64_tr_b16 v[90:91], v135 offset:768\n"
    "ds_read_b64_tr_b16 v[92:93], v135 offset:64\n"
    "ds_read_b64_tr_b16 v[94:95], v135 offset:832\n"
    "s_waitcnt lgkmcnt(10)\n"
    "v_mfma_f32_32x32x16_bf16 v[16:31], v[64:67], v[48:51], v[16:31]\n"
    "s_waitcnt vmcnt(0)\n"
    "ds_write_b128 v139, v[172:175] offset:16384\n"
    "ds_write_b128 v139, v[176:179] offset:20480\n"
    "ds_write_b128 v139, v[180:183] offset:24576\n"
    "v_mfma_f32_16x16x32_bf16 v[96:99], v[104:107], v[64:67], v[96:99]\n"
    "v_mfma_f32_32x32x16_bf16 v[32:47], v[68:71], v[48:51], v[32:47]\n"
    "v_mfma_f32_16x16x32_bf16 v[100:103], v[104:107], v[68:71], v[100:103]\n"
    "v_mfma_f32_32x32x16_bf16 v[16:31], v[72:75], v[52:55], v[16:31]\n"
    "s_add_u32 s58, s58, 0x4000\n"
    "s_and_b32 s58, s58, 0x1ffff\n"
    "v_add_u32 v108, s58, v128\n"
    "v_add_u32 v109, s58, v129\n"
    "v_add_u32 v110, s58, v130\n"
    "v_add_u32 v111, s58, v131\n"
    "v_mfma_f32_32x32x16_bf16 v[32:47], v[76:79], v[52:55], v[32:47]\n"
    "s_waitcnt lgkmcnt(0)\n"
    "s_barrier\n"
    "v_mfma_f32_32x32x16_bf16 v[16:31], v[80:83], v[56:59], v[16:31]\n"
    "ds_read_b128 v[48:51], v108\n"
    "ds_read_b64_tr_b16 v[64:65], v132 offset:16384\n"
    "ds_read_b64_tr_b16 v[66:67], v132 offset:17152\n"
    "ds_read_b64_tr_b16 v[68:69], v132 offset:16448\n"
    "ds_read_b64_tr_b16 v[70:71], v132 offset:17216\n"
    "v_mfma_f32_32x32x16_bf16 v[32:47], v[84:87], v[56:59], v[32:47]\n"
    "ds_read_b128 v[52:55], v109\n"
    "ds_read_b64_tr_b16 v[72:73], v133 offset:16384\n"
    "ds_read_b64_tr_b16 v[74:75], v133 offset:17152\n"
    "ds_read_b64_tr_b16 v[76:77], v133 offset:16448\n"
    "ds_read_b64_tr_b16 v[78:79], v133 offset:17216\n"
    "v_mfma_f32_32x32x16_bf16 v[16:31], v[88:91], v[60:63], v[16:31]\n"
    "v_mfma_f32_32x32x16_bf16 v[32:47], v[92:95], v[60:63], v[32:47]\n"
    "ds_read_b128 v[56:59], v110\n"
    "ds_read_b64_tr_b16 v[80:81], v134 offset:16384\n"
    "ds_read_b64_tr_b16 v[82:83], v134 offset:17152\n"
    "ds_read_b64_tr_b16 v[84:85], v134 offset:16448\n"
    "ds_read_b64_tr_b16 v[86:87], v134 offset:17216\n"
    "s_waitcnt lgkmcnt(10)\n"
    "v_mfma_f32_32x32x16_bf16 v[16:31], v[64:67], v[48:51], v[16:31]\n"
    "v_mfma_f32_16x16x32_bf16 v[96:99], v[104:107], v[64:67], v[96:99]\n"
    "ds_read_b128 v[60:63], v111\n"
    "ds_read_b64_tr_b16 v[88:89], v135 offset:16384\n"
    "ds_read_b64_tr_b16 v[90:91], v135 offset:17152\n"
    "ds_read_b64_tr_b16 v[92:93], v135 offset:16448\n"
    "ds_read_b64_tr_b16 v[94:95], v135 offset:17216\n"
    "v_mfma_f32_32x32x16_bf16 v[32:47], v[68:71], v[48:51], v[32:47]\n"
    "v_mfma_f32_16x16x32_bf16 v[100:103], v[104:107], v[68:71], v[100:103]\n"
    "s_waitcnt lgkmcnt(10)\n"
    "v_mfma_f32_32x32x16_bf16 v[16:31], v[72:75], v[52:55], v[16:31]\n"
    "v_mfma_f32_32x32x16_bf16 v[32:47], v[76:79], v[52:55], v[32:47]\n"
    "s_waitcnt lgkmcnt(0)\n"
    "v_mfma_f32_32x32x16_bf16 v[16:31], v[80:83], v[56:59], v[16:31]\n"
    "s_nop 3\n"
    "v_permlane16_swap_b32 v96, v97\n"
    "v_cvt_pk_bf16_f32 v114, v96, v96\n"
    "v_lshlrev_b32 v114, 16, v114\n"
    "v_sub_f32 v114, v96, v114\n"
    "v_cvt_pk_bf16_f32 v114, v96, v114\n"
    "v_mfma_f32_32x32x16_bf16 v[32:47], v[84:87], v[56:59], v[32:47]\n"
    "v_permlane16_swap_b32 v100, v101\n"
    "v_cvt_pk_bf16_f32 v115, v100, v100\n"
    "v_lshlrev_b32 v115, 16, v115\n"
    "v_sub_f32 v115, v100, v115\n"
    "v_cvt_pk_bf16_f32 v115, v100, v115\n"
    "v_mfma_f32_32x32x16_bf16 v[16:31], v[88:91], v[60:63], v[16:31]\n"
    "s_mov_b64 exec, 0xffffffff\n"
    "ds_write_b32 v142, v114\n"
    "ds_write_b32 v142, v115 offset:512\n"
    "s_mov_b64 exec, -1\n"
    "v_mfma_f32_32x32x16_bf16 v[32:47], v[92:95], v[60:63], v[32:47]\n"
    "s_waitcnt lgkmcnt(0)\n"
    "s_barrier\n"
    "ds_read_b32 v48, v144\n"
    "ds_read_b32 v49, v144\n"
    "ds_read_b32 v50, v144\n"
    "ds_read_b32 v51, v144\n"
    "ds_read_b128 v[52:55], v143 offset:0\n"
    "ds_read_b128 v[56:59], v143 offset:512\n"
    "ds_read_b32 v64, v145\n"
    "s_waitcnt lgkmcnt(1)\n"
    "v_mfma_f32_32x32x16_bf16 v[16:31], v[52:55], v[48:51], v[16:31]\n"
    "v_mfma_f32_32x32x16_bf16 v[32:47], v[56:59], v[48:51], v[32:47]\n"
    "s_waitcnt lgkmcnt(0)\n"
    "v_mul_f32 v66, s77, v64\n"
    "v_mul_f32 v64, s76, v64\n"
    "s_nop 8\n"
    "v_pk_mul_f32 v[16:17], v[16:17], v[64:65] op_sel_hi:[1,0]\n"
    "v_pk_mul_f32 v[18:19], v[18:19], v[64:65] op_sel_hi:[1,0]\n"
    "v_pk_mul_f32 v[20:21], v[20:21], v[64:65] op_sel_hi:[1,0]\n"
    "v_pk_mul_f32 v[22:23], v[22:23], v[64:65] op_sel_hi:[1,0]\n"
    "v_cvt_pk_bf16_f32 v80, v16, v17\n"
    "v_cvt_pk_bf16_f32 v81, v18, v19\n"
    "v_cvt_pk_bf16_f32 v82, v20, v21\n"
    "v_cvt_pk_bf16_f32 v83, v22, v23\n"
    "global_store_dwordx4 v147, v[80:83], s[72:73] sc1\n"
    "v_pk_mul_f32 v[24:25], v[24:25], v[64:65] op_sel_hi:[1,0]\n"
    "v_pk_mul_f32 v[26:27], v[26:27], v[64:65] op_sel_hi:[1,0]\n"
    "v_pk_mul_f32 v[28:29], v[28:29], v[64:65] op_sel_hi:[1,0]\n"
    "v_pk_mul_f32 v[30:31], v[30:31], v[64:65] op_sel_hi:[1,0]\n"
    "v_cvt_pk_bf16_f32 v84, v24, v25\n"
    "v_cvt_pk_bf16_f32 v85, v26, v27\n"
    "v_cvt_pk_bf16_f32 v86, v28, v29\n"
    "v_cvt_pk_bf16_f32 v87, v30, v31\n"
    "global_store_dwordx4 v148, v[84:87], s[72:73] sc1\n"
    "v_pk_mul_f32 v[32:33], v[32:33], v[66:67] op_sel_hi:[1,0]\n"
    "v_pk_mul_f32 v[34:35], v[34:35], v[66:67] op_sel_hi:[1,0]\n"
    "v_pk_mul_f32 v[36:37], v[36:37], v[66:67] op_sel_hi:[1,0]\n"
    "v_pk_mul_f32 v[38:39], v[38:39], v[66:67] op_sel_hi:[1,0]\n"
    "v_cvt_pk_bf16_f32 v88, v32, v33\n"
    "v_cvt_pk_bf16_f32 v89, v34, v35\n"
    "v_cvt_pk_bf16_f32 v90, v36, v37\n"
    "v_cvt_pk_bf16_f32 v91, v38, v39\n"
    "global_store_dwordx4 v149, v[88:91], s[74:75] sc1\n"
    "v_pk_mul_f32 v[40:41], v[40:41], v[66:67] op_sel_hi:[1,0]\n"
    "v_pk_mul_f32 v[42:43], v[42:43], v[66:67] op_sel_hi:[1,0]\n"
    "v_pk_mul_f32 v[44:45], v[44:45], v[66:67] op_sel_hi:[1,0]\n"
    "v_pk_mul_f32 v[46:47], v[46:47], v[66:67] op_sel_hi:[1,0]\n"
    "v_cvt_pk_bf16_f32 v92, v40, v41\n"
    "v_cvt_pk_bf16_f32 v93, v42, v43\n"
    "v_cvt_pk_bf16_f32 v94, v44, v45\n"
    "v_cvt_pk_bf16_f32 v95, v46, v47\n"
    "global_store_dwordx4 v150, v[92:95], s[74:75] sc1\n"
    "v_mov_b32 v114, s88\n"
    "v_mov_b32 v115, s89\n"
    "s_mov_b64 exec, 1\n"
    "s_waitcnt vmcnt(0)\n"
    "ds_add_rtn_u32 v116, v154, v156\n"
    "s_waitcnt lgkmcnt(0)\n"
    "v_readfirstlane_b32 s59, v116\n"
    "s_mov_b64 exec, 0x3f\n"
    "s_cmp_eq_u32 s59, 7\n"
    "s_cbranch_scc0 2f\n"
    "global_store_dwordx2 v151, v[114:115], s[70:71] sc1\n"
    "2:\n"
    "s_mov_b64 exec, -1\n"
                P1_OPERANDS);
        } else {
            if (vt) asm volatile(
    "v_mov_b64 v[16:17], 0\n"
    "v_mov_b64 v[18:19], 0\n"
    "v_mov_b64 v[20:21], 0\n"
    "v_mov_b64 v[22:23], 0\n"
    "v_mov_b64 v[24:25], 0\n"
    "v_mov_b64 v[26:27], 0\n"
    "v_mov_b64 v[28:29], 0\n"
    "v_mov_b64 v[30:31], 0\n"
    "v_mov_b64 v[56:57], 0\n"
    "v_mov_b64 v[58:59], 0\n"
    "v_mov_b64 v[60:61], 0\n"
    "v_mov_b64 v[62:63], 0\n"
    "v_mov_b64 v[80:81], 0\n"
    "v_mov_b64 v[82:83], 0\n"
    "v_mov_b64 v[84:85], 0\n"
    "v_mov_b64 v[86:87], 0\n"
    "v_mov_b64 v[88:89], 0\n"
    "v_mov_b64 v[90:91], 0\n"
    "v_mov_b64 v[92:93], 0\n"
    "v_mov_b64 v[94:95], 0\n"
    "v_mov_b64 v[96:97], 0\n"
    "v_mov_b64 v[98:99], 0\n"
    "v_mov_b64 v[100:101], 0\n"
    "v_mov_b64 v[102:103], 0\n"
    "v_mov_b32 v104, v140\n"
    "v_mov_b32 v105, v140\n"
    "v_mov_b32 v106, v140\n"
    "v_mov_b32 v107, v140\n"
    "s_mov_b32 s56, 0\n"
    "s_lshl_b32 s58, s66, 14\n"
    "s_add_u32 s57, s66, 1\n"
    "s_add_u32 s57, s57, 1\n"
    "s_and_b32 s57, s57, 7\n"
    "s_lshl_b32 s59, s57, 7\n"
    "s_lshl_b32 s60, s57, 14\n"
    "s_add_u32 s60, s60, s67\n"
    "s_add_u32 s50, s64, s59\n"
    "s_addc_u32 s51, s65, 0\n"
    "v_add_u32 v108, s58, v128\n"
    "v_add_u32 v109, s58, v129\n"
    "v_add_u32 v110, s58, v130\n"
    "v_add_u32 v111, s58, v131\n"
    "s_setprio 1\n"
    "s_waitcnt vmcnt(5) lgkmcnt(0)\n"
    "s_barrier\n"
    "1:\n"
    "v_mfma_f32_32x32x16_bf16 v[16:31], v[56:59], v[80:83], v[16:31]\n"
    "ds_read_b128 v[48:51], v108\n"
    "ds_read_b64_tr_b16 v[64:65], v132 offset:128\n"
    "ds_read_b64_tr_b16 v[66:67], v132 offset:896\n"
    "s_add_u32 m0, s60, 0\n"
    "ds_read_b128 v[52:55], v109\n"
    "ds_read_b64_tr_b16 v[72:73], v133 offset:128\n"
    "ds_read_b64_tr_b16 v[74:75], v133 offset:896\n"
    "global_load_lds_dwordx4 v136, s[50:51]\n"
    "s_add_u32 m0, s60, 1024\n"
    "v_mfma_f32_32x32x16_bf16 v[16:31], v[60:63], v[88:91], v[16:31]\n"
    "global_load_lds_dwordx4 v137, s[50:51]\n"
    "ds_read_b128 v[56:59], v110\n"
    "ds_read_b64_tr_b16 v[80:81], v134 offset:128\n"
    "ds_read_b64_tr_b16 v[82:83], v134 offset:896\n"
    "ds_read_b128 v[60:63], v111\n"
    "ds_read_b64_tr_b16 v[88:89], v135 offset:128\n"
    "ds_read_b64_tr_b16 v[90:91], v135 offset:896\n"
    "s_add_u32 m0, s60, 2048\n"
    "s_waitcnt lgkmcnt(9)\n"
    "v_mfma_f32_32x32x16_bf16 v[16:31], v[48:51], v[64:67], v[16:31]\n"
    "global_load_lds_dwordx4 v138, s[50:51]\n"
    "v_mfma_f32_16x16x32_bf16 v[96:99], v[104:107], v[64:67], v[96:99]\n"
    "s_add_u32 m0, s60, 3072\n"
    "s_waitcnt lgkmcnt(6)\n"
    "v_mfma_f32_32x32x16_bf16 v[16:31], v[52:55], v[72:75], v[16:31]\n"
    "global_load_lds_dwordx4 v139, s[50:51]\n"
    "s_add_u32 s58, s58, 0x4000\n"
    "s_and_b32 s58, s58, 0x1ffff\n"
    "v_add_u32 v108, s58, v128\n"
    "v_add_u32 v109, s58, v129\n"
    "v_add_u32 v110, s58, v130\n"
    "v_add_u32 v111, s58, v131\n"
    "s_add_u32 s57, s57, 1\n"
    "s_and_b32 s57, s57, 7\n"
    "s_lshl_b32 s59, s57, 7\n"
    "s_lshl_b32 s60, s57, 14\n"
    "s_add_u32 s60, s60, s67\n"
    "s_add_u32 s50, s64, s59\n"
    "s_addc_u32 s51, s65, 0\n"
    "s_waitcnt vmcnt(4) lgkmcnt(0)\n"
    "s_barrier\n"
    "v_mfma_f32_32x32x16_bf16 v[16:31], v[56:59], v[80:83], v[16:31]\n"
    "ds_read_b128 v[48:51], v108\n"
    "ds_read_b64_tr_b16 v[64:65], v132 offset:16512\n"
    "ds_read_b64_tr_b16 v[66:67], v132 offset:17280\n"
    "s_add_u32 m0, s60, 0\n"
    "ds_read_b128 v[52:55], v109\n"
    "ds_read_b64_tr_b16 v[72:73], v133 offset:16512\n"
    "ds_read_b64_tr_b16 v[74:75], v133 offset:17280\n"
    "global_load_lds_dwordx4 v136, s[50:51]\n"
    "s_add_u32 m0, s60, 1024\n"
    "v_mfma_f32_32x32x16_bf16 v[16:31], v[60:63], v[88:91], v[16:31]\n"
    "global_load_lds_dwordx4 v137, s[50:51]\n"
    "ds_read_b128 v[56:59], v110\n"
    "ds_read_b64_tr_b16 v[80:81], v134 offset:16512\n"
    "ds_read_b64_tr_b16 v[82:83], v134 offset:17280\n"
    "ds_read_b128 v[60:63], v111\n"
    "ds_read_b64_tr_b16 v[88:89], v135 offset:16512\n"
    "ds_read_b64_tr_b16 v[90:91], v135 offset:17280\n"
    "s_add_u32 m0, s60, 2048\n"
    "s_waitcnt lgkmcnt(9)\n"
    "v_mfma_f32_32x32x16_bf16 v[16:31], v[48:51], v[64:67], v[16:31]\n"
    "global_load_lds_dwordx4 v138, s[50:51]\n"
    "v_mfma_f32_16x16x32_bf16 v[96:99], v[104:107], v[64:67], v[96:99]\n"
    "s_add_u32 m0, s60, 3072\n"
    "s_waitcnt lgkmcnt(6)\n"
    "v_mfma_f32_32x32x16_bf16 v[16:31], v[52:55], v[72:75], v[16:31]\n"
    "global_load_lds_dwordx4 v139, s[50:51]\n"
    "s_add_u32 s58, s58, 0x4000\n"
    "s_and_b32 s58, s58, 0x1ffff\n"
    "v_add_u32 v108, s58, v128\n"
    "v_add_u32 v109, s58, v129\n"
    "v_add_u32 v110, s58, v130\n"
    "v_add_u32 v111, s58, v131\n"
    "s_add_u32 s57, s57, 1\n"
    "s_and_b32 s57, s57, 7\n"
    "s_lshl_b32 s59, s57, 7\n"
    "s_lshl_b32 s60, s57, 14\n"
    "s_add_u32 s60, s60, s67\n"
    "s_add_u32 s50, s64, s59\n"
    "s_addc_u32 s51, s65, 0\n"
    "s_add_u32 s56, s56, 1\n"
    "s_cmp_lt_u32 s56, 3\n"
    "s_waitcnt vmcnt(4) lgkmcnt(0)\n"
    "s_barrier\n"
    "s_cbranch_scc1 1b\n"
    "s_mov_b32 m0, s78\n"
    "global_load_dwordx4 v[116:119], v155, s[70:71] sc1\n"
    "global_load_lds_dword v141, s[68:69] sc1\n"
    "v_mfma_f32_32x32x16_bf16 v[16:31], v[56:59], v[80:83], v[16:31]\n"
    "ds_read_b128 v[48:51], v108\n"
    "ds_read_b64_tr_b16 v[64:65], v132 offset:128\n"
    "ds_read_b64_tr_b16 v[66:67], v132 offset:896\n"
    "ds_read_b128 v[52:55], v109\n"
    "ds_read_b64_tr_b16 v[72:73], v133 offset:128\n"
    "ds_read_b64_tr_b16 v[74:75], v133 offset:896\n"
    "v_mfma_f32_32x32x16_bf16 v[16:31], v[60:63], v[88:91], v[16:31]\n"
    "ds_read_b128 v[56:59], v110\n"
    "ds_read_b64_tr_b16 v[80:81], v134 offset:128\n"
    "ds_read_b64_tr_b16 v[82:83], v134 offset:896\n"
    "ds_read_b128 v[60:63], v111\n"
    "ds_read_b64_tr_b16 v[88:89], v135 offset:128\n"
    "ds_read_b64_tr_b16 v[90:91], v135 offset:896\n"
    "s_waitcnt lgkmcnt(9)\n"
    "v_mfma_f32_32x32x16_bf16 v[16:31], v[48:51], v[64:67], v[16:31]\n"
    "v_mfma_f32_16x16x32_bf16 v[96:99], v[104:107], v[64:67], v[96:99]\n"
    "s_waitcnt lgkmcnt(6)\n"
    "v_mfma_f32_32x32x16_bf16 v[16:31], v[52:55], v[72:75], v[16:31]\n"
    "s_add_u32 s58, s58, 0x4000\n"
    "s_and_b32 s58, s58, 0x1ffff\n"
    "v_add_u32 v108, s58, v128\n"
    "v_add_u32 v109, s58, v129\n"
    "v_add_u32 v110, s58, v130\n"
    "v_add_u32 v111, s58, v131\n"
    "s_waitcnt vmcnt(2) lgkmcnt(0)\n"
    "s_barrier\n"
    "v_mfma_f32_32x32x16_bf16 v[16:31], v[56:59], v[80:83], v[16:31]\n"
    "ds_read_b128 v[48:51], v108\n"
    "ds_read_b64_tr_b16 v[64:65], v132 offset:16512\n"
    "ds_read_b64_tr_b16 v[66:67], v132 offset:17280\n"
    "ds_read_b128 v[52:55], v109\n"
    "ds_read_b64_tr_b16 v[72:73], v133 offset:16512\n"
    "ds_read_b64_tr_b16 v[74:75], v133 offset:17280\n"
    "v_mfma_f32_32x32x16_bf16 v[16:31], v[60:63], v[88:91], v[16:31]\n"
    "ds_read_b128 v[56:59], v110\n"
    "ds_read_b64_tr_b16 v[80:81], v134 offset:16512\n"
    "ds_read_b64_tr_b16 v[82:83], v134 offset:17280\n"
    "ds_read_b128 v[60:63], v111\n"
    "ds_read_b64_tr_b16 v[88:89], v135 offset:16512\n"
    "ds_read_b64_tr_b16 v[90:91], v135 offset:17280\n"
    "s_mov_b32 s59, 0x40000\n"
    "s_waitcnt vmcnt(1)\n"
    "3:\n"
    "v_cmp_ne_u64 vcc, s[88:89], v[118:119]\n"
    "s_cbranch_vccz 4f\n"
    "s_sub_u32 s59, s59, 1\n"
    "s_cbranch_scc1 4f\n"
    "global_load_dwordx4 v[116:119], v155, s[70:71] sc1\n"
    "s_waitcnt vmcnt(0)\n"
    "s_branch 3b\n"
    "4:\n"
    "v_rcp_f32 v116, v116\n"
    "s_waitcnt lgkmcnt(9)\n"
    "v_mfma_f32_32x32x16_bf16 v[16:31], v[48:51], v[64:67], v[16:31]\n"
    "v_mfma_f32_16x16x32_bf16 v[96:99], v[104:107], v[64:67], v[96:99]\n"
    "s_waitcnt lgkmcnt(6)\n"
    "v_mfma_f32_32x32x16_bf16 v[16:31], v[52:55], v[72:75], v[16:31]\n"
    "s_waitcnt lgkmcnt(3)\n"
    "v_mfma_f32_32x32x16_bf16 v[16:31], v[56:59], v[80:83], v[16:31]\n"
    "s_waitcnt lgkmcnt(0)\n"
    "ds_write_b32 v146, v116\n"
    "ds_write_b32 v146, v117 offset:512\n"
    "s_nop 0\n"
    "v_permlane16_swap_b32 v96, v97\n"
    "v_cvt_pk_bf16_f32 v114, v96, v96\n"
    "v_lshlrev_b32 v114, 16, v114\n"
    "v_sub_f32 v114, v96, v114\n"
    "v_cvt_pk_bf16_f32 v114, v96, v114\n"
    "s_mov_b64 exec, 0xffffffff\n"
    "ds_write_b32 v142, v114 offset:1024\n"
    "s_mov_b64 exec, -1\n"
    "v_mfma_f32_32x32x16_bf16 v[16:31], v[60:63], v[88:91], v[16:31]\n"
    "s_waitcnt lgkmcnt(0)\n"
    "s_barrier\n"
    "ds_read_b32 v48, v144\n"
    "ds_read_b32 v49, v144\n"
    "ds_read_b32 v50, v144\n"
    "ds_read_b32 v51, v144\n"
    "ds_read_b128 v[52:55], v143 offset:1024\n"
    "ds_read_b128 v[64:67], v145 offset:0\n"
    "ds_read_b128 v[68:71], v145 offset:32\n"
    "ds_read_b128 v[72:75], v145 offset:64\n"
    "ds_read_b128 v[76:79], v145 offset:96\n"
    "s_waitcnt lgkmcnt(4)\n"
    "v_mfma_f32_32x32x16_bf16 v[16:31], v[48:51], v[52:55], v[16:31]\n"
    "s_waitcnt lgkmcnt(0)\n"
    "s_nop 10\n"
    "v_pk_mul_f32 v[16:17], v[16:17], v[64:65]\n"
    "v_pk_mul_f32 v[18:19], v[18:19], v[66:67]\n"
    "v_pk_mul_f32 v[20:21], v[20:21], v[68:69]\n"
    "v_pk_mul_f32 v[22:23], v[22:23], v[70:71]\n"
    "v_cvt_pk_bf16_f32 v80, v16, v17\n"
    "v_cvt_pk_bf16_f32 v81, v18, v19\n"
    "v_cvt_pk_bf16_f32 v82, v20, v21\n"
    "v_cvt_pk_bf16_f32 v83, v22, v23\n"
    "global_store_dwordx4 v147, v[80:83], s[72:73] sc1\n"
    "v_pk_mul_f32 v[24:25], v[24:25], v[72:73]\n"
    "v_pk_mul_f32 v[26:27], v[26:27], v[74:75]\n"
    "v_pk_mul_f32 v[28:29], v[28:29], v[76:77]\n"
    "v_pk_mul_f32 v[30:31], v[30:31], v[78:79]\n"
    "v_cvt_pk_bf16_f32 v84, v24, v25\n"
    "v_cvt_pk_bf16_f32 v85, v26, v27\n"
    "v_cvt_pk_bf16_f32 v86, v28, v29\n"
    "v_cvt_pk_bf16_f32 v87, v30, v31\n"
    "global_store_dwordx4 v148, v[84:87], s[72:73] sc1\n"
    "s_setprio 0\n"
    "s_sleep 3\n"
    "s_mov_b64 exec, s[86:87]\n"
    "s_lshr_b32 s59, s85, 1\n"
    "s_mov_b32 m0, s84\n"
    "s_add_u32 s58, m0, s59\n"
    "global_load_lds_dwordx4 v152, s[80:81]\n"
    "s_mov_b32 m0, s58\n"
    "s_add_u32 s80, s80, 57344\n"
    "s_addc_u32 s81, s81, 0\n"
    "global_load_lds_dwordx4 v153, s[82:83]\n"
    "s_add_u32 m0, s58, s59\n"
    "s_add_u32 s82, s82, 57344\n"
    "s_addc_u32 s83, s83, 0\n"
    "s_add_u32 s58, m0, s59\n"
    "global_load_lds_dwordx4 v152, s[80:81]\n"
    "s_mov_b32 m0, s58\n"
    "s_add_u32 s80, s80, 57344\n"
    "s_addc_u32 s81, s81, 0\n"
    "global_load_lds_dwordx4 v153, s[82:83]\n"
    "s_add_u32 m0, s58, s59\n"
    "s_add_u32 s82, s82, 57344\n"
    "s_addc_u32 s83, s83, 0\n"
    "s_add_u32 s58, m0, s59\n"
    "global_load_lds_dwordx4 v152, s[80:81]\n"
    "s_mov_b32 m0, s58\n"
    "s_add_u32 s80, s80, 57344\n"
    "s_addc_u32 s81, s81, 0\n"
    "global_load_lds_dwordx4 v153, s[82:83]\n"
    "s_add_u32 m0, s58, s59\n"
    "s_add_u32 s82, s82, 57344\n"
    "s_addc_u32 s83, s83, 0\n"
    "s_add_u32 s58, m0, s59\n"
    "global_load_lds_dwordx4 v152, s[80:81]\n"
    "s_mov_b32 m0, s58\n"
    "s_add_u32 s80, s80, 57344\n"
    "s_addc_u32 s81, s81, 0\n"
    "global_load_lds_dwordx4 v153, s[82:83]\n"
    "s_add_u32 m0, s58, s59\n"
    "s_add_u32 s82, s82, 57344\n"
    "s_addc_u32 s83, s83, 0\n"
    "v_mov_b32 v114, s88\n"
    "v_mov_b32 v115, s89\n"
    "s_mov_b64 exec, 1\n"
    "s_waitcnt vmcnt(8)\n"
    "ds_add_rtn_u32 v116, v154, v156\n"
    "s_waitcnt lgkmcnt(0)\n"
    "v_readfirstlane_b32 s59, v116\n"
    "s_mov_b64 exec, 0x3f\n"
    "s_cmp_eq_u32 s59, 7\n"
    "s_cbranch_scc0 2f\n"
    "global_store_dwordx2 v151, v[114:115], s[70:71] sc1\n"
    "2:\n"
    "s_mov_b64 exec, -1\n"
                P1_OPERANDS);
            else asm volatile(
    "v_mov_b64 v[16:17], 0\n"
    "v_mov_b64 v[18:19], 0\n"
    "v_mov_b64 v[20:21], 0\n"
    "v_mov_b64 v[22:23], 0\n"
    "v_mov_b64 v[24:25], 0\n"
    "v_mov_b64 v[26:27], 0\n"
    "v_mov_b64 v[28:29], 0\n"
    "v_mov_b64 v[30:31], 0\n"
    "v_mov_b64 v[56:57], 0\n"
    "v_mov_b64 v[58:59], 0\n"
    "v_mov_b64 v[60:61], 0\n"
    "v_mov_b64 v[62:63], 0\n"
    "v_mov_b64 v[80:81], 0\n"
    "v_mov_b64 v[82:83], 0\n"
    "v_mov_b64 v[84:85], 0\n"
    "v_mov_b64 v[86:87], 0\n"
    "v_mov_b64 v[88:89], 0\n"
    "v_mov_b64 v[90:91], 0\n"
    "v_mov_b64 v[92:93], 0\n"
    "v_mov_b64 v[94:95], 0\n"
    "v_mov_b64 v[96:97], 0\n"
    "v_mov_b64 v[98:99], 0\n"
    "v_mov_b64 v[100:101], 0\n"
    "v_mov_b64 v[102:103], 0\n"
    "v_mov_b32 v104, v140\n"
    "v_mov_b32 v105, v140\n"
    "v_mov_b32 v106, v140\n"
    "v_mov_b32 v107, v140\n"
    "s_mov_b32 s56, 0\n"
    "s_lshl_b32 s58, s66, 14\n"
    "s_add_u32 s57, s66, 1\n"
    "s_add_u32 s57, s57, 1\n"
    "s_and_b32 s57, s57, 7\n"
    "s_lshl_b32 s59, s57, 7\n"
    "s_lshl_b32 s60, s57, 14\n"
    "s_add_u32 s60, s60, s67\n"
    "s_add_u32 s50, s64, s59\n"
    "s_addc_u32 s51, s65, 0\n"
    "v_add_u32 v108, s58, v128\n"
    "v_add_u32 v109, s58, v129\n"
    "v_add_u32 v110, s58, v130\n"
    "v_add_u32 v111, s58, v131\n"
    "s_setprio 1\n"
    "s_waitcnt vmcnt(5) lgkmcnt(0)\n"
    "s_barrier\n"
    "1:\n"
    "v_mfma_f32_32x32x16_bf16 v[16:31], v[80:83], v[56:59], v[16:31]\n"
    "ds_read_b128 v[48:51], v108\n"
    "ds_read_b64_tr_b16 v[64:65], v132 offset:128\n"
    "ds_read_b64_tr_b16 v[66:67], v132 offset:896\n"
    "s_add_u32 m0, s60, 0\n"
    "ds_read_b128 v[52:55], v109\n"
    "ds_read_b64_tr_b16 v[72:73], v133 offset:128\n"
    "ds_read_b64_tr_b16 v[74:75], v133 offset:896\n"
    "global_load_lds_dwordx4 v136, s[50:51]\n"
    "s_add_u32 m0, s60, 1024\n"
    "v_mfma_f32_32x32x16_bf16 v[16:31], v[88:91], v[60:63], v[16:31]\n"
    "global_load_lds_dwordx4 v137, s[50:51]\n"
    "ds_read_b128 v[56:59], v110\n"
    "ds_read_b64_tr_b16 v[80:81], v134 offset:128\n"
    "ds_read_b64_tr_b16 v[82:83], v134 offset:896\n"
    "ds_read_b128 v[60:63], v111\n"
    "ds_read_b64_tr_b16 v[88:89], v135 offset:128\n"
    "ds_read_b64_tr_b16 v[90:91], v135 offset:896\n"
    "s_add_u32 m0, s60, 2048\n"
    "s_waitcnt lgkmcnt(9)\n"
    "v_mfma_f32_32x32x16_bf16 v[16:31], v[64:67], v[48:51], v[16:31]\n"
    "global_load_lds_dwordx4 v138, s[50:51]\n"
    "v_mfma_f32_16x16x32_bf16 v[96:99], v[104:107], v[64:67], v[96:99]\n"
    "s_add_u32 m0, s60, 3072\n"
    "s_waitcnt lgkmcnt(6)\n"
    "v_mfma_f32_32x32x16_bf16 v[16:31], v[72:75], v[52:55], v[16:31]\n"
    "global_load_lds_dwordx4 v139, s[50:51]\n"
    "s_add_u32 s58, s58, 0x4000\n"
    "s_and_b32 s58, s58, 0x1ffff\n"
    "v_add_u32 v108, s58, v128\n"
    "v_add_u32 v109, s58, v129\n"
    "v_add_u32 v110, s58, v130\n"
    "v_add_u32 v111, s58, v131\n"
    "s_add_u32 s57, s57, 1\n"
    "s_and_b32 s57, s57, 7\n"
    "s_lshl_b32 s59, s57, 7\n"
    "s_lshl_b32 s60, s57, 14\n"
    "s_add_u32 s60, s60, s67\n"
    "s_add_u32 s50, s64, s59\n"
    "s_addc_u32 s51, s65, 0\n"
    "s_waitcnt vmcnt(4) lgkmcnt(0)\n"
    "s_barrier\n"
    "v_mfma_f32_32x32x16_bf16 v[16:31], v[80:83], v[56:59], v[16:31]\n"
    "ds_read_b128 v[48:51], v108\n"
    "ds_read_b64_tr_b16 v[64:65], v132 offset:16512\n"
    "ds_read_b64_tr_b16 v[66:67], v132 offset:17280\n"
    "s_add_u32 m0, s60, 0\n"
    "ds_read_b128 v[52:55], v109\n"
    "ds_read_b64_tr_b16 v[72:73], v133 offset:16512\n"
    "ds_read_b64_tr_b16 v[74:75], v133 offset:17280\n"
    "global_load_lds_dwordx4 v136, s[50:51]\n"
    "s_add_u32 m0, s60, 1024\n"
    "v_mfma_f32_32x32x16_bf16 v[16:31], v[88:91], v[60:63], v[16:31]\n"
    "global_load_lds_dwordx4 v137, s[50:51]\n"
    "ds_read_b128 v[56:59], v110\n"
    "ds_read_b64_tr_b16 v[80:81], v134 offset:16512\n"
    "ds_read_b64_tr_b16 v[82:83], v134 offset:17280\n"
    "ds_read_b128 v[60:63], v111\n"
    "ds_read_b64_tr_b16 v[88:89], v135 offset:16512\n"
    "ds_read_b64_tr_b16 v[90:91], v135 offset:17280\n"
    "s_add_u32 m0, s60, 2048\n"
    "s_waitcnt lgkmcnt(9)\n"
    "v_mfma_f32_32x32x16_bf16 v[16:31], v[64:67], v[48:51], v[16:31]\n"
    "global_load_lds_dwordx4 v138, s[50:51]\n"
    "v_mfma_f32_16x16x32_bf16 v[96:99], v[104:107], v[64:67], v[96:99]\n"
    "s_add_u32 m0, s60, 3072\n"
    "s_waitcnt lgkmcnt(6)\n"
    "v_mfma_f32_32x32x16_bf16 v[16:31], v[72:75], v[52:55], v[16:31]\n"
    "global_load_lds_dwordx4 v139, s[50:51]\n"
    "s_add_u32 s58, s58, 0x4000\n"
    "s_and_b32 s58, s58, 0x1ffff\n"
    "v_add_u32 v108, s58, v128\n"
    "v_add_u32 v109, s58, v129\n"
    "v_add_u32 v110, s58, v130\n"
    "v_add_u32 v111, s58, v131\n"
    "s_add_u32 s57, s57, 1\n"
    "s_and_b32 s57, s57, 7\n"
    "s_lshl_b32 s59, s57, 7\n"
    "s_lshl_b32 s60, s57, 14\n"
    "s_add_u32 s60, s60, s67\n"
    "s_add_u32 s50, s64, s59\n"
    "s_addc_u32 s51, s65, 0\n"
    "s_add_u32 s56, s56, 1\n"
    "s_cmp_lt_u32 s56, 3\n"
    "s_waitcnt vmcnt(4) lgkmcnt(0)\n"
    "s_barrier\n"
    "s_cbranch_scc1 1b\n"
    "s_mov_b32 m0, s78\n"
    "global_load_dwordx4 v[116:119], v155, s[70:71] sc1\n"
    "global_load_lds_dword v141, s[68:69] sc1\n"
    "v_mfma_f32_32x32x16_bf16 v[16:31], v[80:83], v[56:59], v[16:31]\n"
    "ds_read_b128 v[48:51], v108\n"
    "ds_read_b64_tr_b16 v[64:65], v132 offset:128\n"
    "ds_read_b64_tr_b16 v[66:67], v132 offset:896\n"
    "ds_read_b128 v[52:55], v109\n"
    "ds_read_b64_tr_b16 v[72:73], v133 offset:128\n"
    "ds_read_b64_tr_b16 v[74:75], v133 offset:896\n"
    "v_mfma_f32_32x32x16_bf16 v[16:31], v[88:91], v[60:63], v[16:31]\n"
    "ds_read_b128 v[56:59], v110\n"
    "ds_read_b64_tr_b16 v[80:81], v134 offset:128\n"
    "ds_read_b64_tr_b16 v[82:83], v134 offset:896\n"
    "ds_read_b128 v[60:63], v111\n"
    "ds_read_b64_tr_b16 v[88:89], v135 offset:128\n"
    "ds_read_b64_tr_b16 v[90:91], v135 offset:896\n"
    "s_waitcnt lgkmcnt(9)\n"
    "v_mfma_f32_32x32x16_bf16 v[16:31], v[64:67], v[48:51], v[16:31]\n"
    "v_mfma_f32_16x16x32_bf16 v[96:99], v[104:107], v[64:67], v[96:99]\n"
    "s_waitcnt lgkmcnt(6)\n"
    "v_mfma_f32_32x32x16_bf16 v[16:31], v[72:75], v[52:55], v[16:31]\n"
    "s_add_u32 s58, s58, 0x4000\n"
    "s_and_b32 s58, s58, 0x1ffff\n"
    "v_add_u32 v108, s58, v128\n"
    "v_add_u32 v109, s58, v129\n"
    "v_add_u32 v110, s58, v130\n"
    "v_add_u32 v111, s58, v131\n"
    "s_waitcnt vmcnt(2) lgkmcnt(0)\n"
    "s_barrier\n"
    "v_mfma_f32_32x32x16_bf16 v[16:31], v[80:83], v[56:59], v[16:31]\n"
    "ds_read_b128 v[48:51], v108\n"
    "ds_read_b64_tr_b16 v[64:65], v132 offset:16512\n"
    "ds_read_b64_tr_b16 v[66:67], v132 offset:17280\n"
    "ds_read_b128 v[52:55], v109\n"
    "ds_read_b64_tr_b16 v[72:73], v133 offset:16512\n"
    "ds_read_b64_tr_b16 v[74:75], v133 offset:17280\n"
    "v_mfma_f32_32x32x16_bf16 v[16:31], v[88:91], v[60:63], v[16:31]\n"
    "ds_read_b128 v[56:59], v110\n"
    "ds_read_b64_tr_b16 v[80:81], v134 offset:16512\n"
    "ds_read_b64_tr_b16 v[82:83], v134 offset:17280\n"
    "ds_read_b128 v[60:63], v111\n"
    "ds_read_b64_tr_b16 v[88:89], v135 offset:16512\n"
    "ds_read_b64_tr_b16 v[90:91], v135 offset:17280\n"
    "s_mov_b32 s59, 0x40000\n"
    "s_waitcnt vmcnt(1)\n"
    "3:\n"
    "v_cmp_ne_u64 vcc, s[88:89], v[118:119]\n"
    "s_cbranch_vccz 4f\n"
    "s_sub_u32 s59, s59, 1\n"
    "s_cbranch_scc1 4f\n"
    "global_load_dwordx4 v[116:119], v155, s[70:71] sc1\n"
    "s_waitcnt vmcnt(0)\n"
    "s_branch 3b\n"
    "4:\n"
    "v_rcp_f32 v116, v116\n"
    "s_waitcnt lgkmcnt(9)\n"
    "v_mfma_f32_32x32x16_bf16 v[16:31], v[64:67], v[48:51], v[16:31]\n"
    "v_mfma_f32_16x16x32_bf16 v[96:99], v[104:107], v[64:67], v[96:99]\n"
    "s_waitcnt lgkmcnt(6)\n"
    "v_mfma_f32_32x32x16_bf16 v[16:31], v[72:75], v[52:55], v[16:31]\n"
    "s_waitcnt lgkmcnt(3)\n"
    "v_mfma_f32_32x32x16_bf16 v[16:31], v[80:83], v[56:59], v[16:31]\n"
    "s_waitcnt lgkmcnt(0)\n"
    "ds_write_b32 v146, v116\n"
    "ds_write_b32 v146, v117 offset:512\n"
    "s_nop 0\n"
    "v_permlane16_swap_b32 v96, v97\n"
    "v_cvt_pk_bf16_f32 v114, v96, v96\n"
    "v_lshlrev_b32 v114, 16, v114\n"
    "v_sub_f32 v114, v96, v114\n"
    "v_cvt_pk_bf16_f32 v114, v96, v114\n"
    "s_mov_b64 exec, 0xffffffff\n"
    "ds_write_b32 v142, v114 offset:1024\n"
    "s_mov_b64 exec, -1\n"
    "v_mfma_f32_32x32x16_bf16 v[16:31], v[88:91], v[60:63], v[16:31]\n"
    "s_waitcnt lgkmcnt(0)\n"
    "s_barrier\n"
    "ds_read_b32 v48, v144\n"
    "ds_read_b32 v49, v144\n"
    "ds_read_b32 v50, v144\n"
    "ds_read_b32 v51, v144\n"
    "ds_read_b128 v[52:55], v143 offset:1024\n"
    "ds_read_b32 v64, v145\n"
    "s_waitcnt lgkmcnt(1)\n"
    "v_mfma_f32_32x32x16_bf16 v[16:31], v[52:55], v[48:51], v[16:31]\n"
    "s_waitcnt lgkmcnt(0)\n"
    "v_mul_f32 v66, s77, v64\n"
    "v_mul_f32 v64, s76, v64\n"
    "s_nop 8\n"
    "v_pk_mul_f32 v[16:17], v[16:17], v[64:65] op_sel_hi:[1,0]\n"
    "v_pk_mul_f32 v[18:19], v[18:19], v[64:65] op_sel_hi:[1,0]\n"
    "v_pk_mul_f32 v[20:21], v[20:21], v[64:65] op_sel_hi:[1,0]\n"
    "v_pk_mul_f32 v[22:23], v[22:23], v[64:65] op_sel_hi:[1,0]\n"
    "v_cvt_pk_bf16_f32 v80, v16, v17\n"
    "v_cvt_pk_bf16_f32 v81, v18, v19\n"
    "v_cvt_pk_bf16_f32 v82, v20, v21\n"
    "v_cvt_pk_bf16_f32 v83, v22, v23\n"
    "global_store_dwordx4 v147, v[80:83], s[72:73] sc1\n"
    "v_pk_mul_f32 v[24:25], v[24:25], v[64:65] op_sel_hi:[1,0]\n"
    "v_pk_mul_f32 v[26:27], v[26:27], v[64:65] op_sel_hi:[1,0]\n"
    "v_pk_mul_f32 v[28:29], v[28:29], v[64:65] op_sel_hi:[1,0]\n"
    "v_pk_mul_f32 v[30:31], v[30:31], v[64:65] op_sel_hi:[1,0]\n"
    "v_cvt_pk_bf16_f32 v84, v24, v25\n"
    "v_cvt_pk_bf16_f32 v85, v26, v27\n"
    "v_cvt_pk_bf16_f32 v86, v28, v29\n"
    "v_cvt_pk_bf16_f32 v87, v30, v31\n"
    "global_store_dwordx4 v148, v[84:87], s[72:73] sc1\n"
    "s_setprio 0\n"
    "s_sleep 3\n"
    "s_mov_b64 exec, s[86:87]\n"
    "s_lshr_b32 s59, s85, 1\n"
    "s_mov_b32 m0, s84\n"
    "s_add_u32 s58, m0, s59\n"
    "global_load_lds_dwordx4 v152, s[80:81]\n"
    "s_mov_b32 m0, s58\n"
    "s_add_u32 s80, s80, 57344\n"
    "s_addc_u32 s81, s81, 0\n"
    "global_load_lds_dwordx4 v153, s[82:83]\n"
    "s_add_u32 m0, s58, s59\n"
    "s_add_u32 s82, s82, 57344\n"
    "s_addc_u32 s83, s83, 0\n"
    "s_add_u32 s58, m0, s59\n"
    "global_load_lds_dwordx4 v152, s[80:81]\n"
    "s_mov_b32 m0, s58\n"
    "s_add_u32 s80, s80, 57344\n"
    "s_addc_u32 s81, s81, 0\n"
    "global_load_lds_dwordx4 v153, s[82:83]\n"
    "s_add_u32 m0, s58, s59\n"
    "s_add_u32 s82, s82, 57344\n"
    "s_addc_u32 s83, s83, 0\n"
    "s_add_u32 s58, m0, s59\n"
    "global_load_lds_dwordx4 v152, s[80:81]\n"
    "s_mov_b32 m0, s58\n"
    "s_add_u32 s80, s80, 57344\n"
    "s_addc_u32 s81, s81, 0\n"
    "global_load_lds_dwordx4 v153, s[82:83]\n"
    "s_add_u32 m0, s58, s59\n"
    "s_add_u32 s82, s82, 57344\n"
    "s_addc_u32 s83, s83, 0\n"
    "s_add_u32 s58, m0, s59\n"
    "global_load_lds_dwordx4 v152, s[80:81]\n"
    "s_mov_b32 m0, s58\n"
    "s_add_u32 s80, s80, 57344\n"
    "s_addc_u32 s81, s81, 0\n"
    "global_load_lds_dwordx4 v153, s[82:83]\n"
    "s_add_u32 m0, s58, s59\n"
    "s_add_u32 s82, s82, 57344\n"
    "s_addc_u32 s83, s83, 0\n"
    "v_mov_b32 v114, s88\n"
    "v_mov_b32 v115, s89\n"
    "s_mov_b64 exec, 1\n"
    "s_waitcnt vmcnt(8)\n"
    "ds_add_rtn_u32 v116, v154, v156\n"
    "s_waitcnt lgkmcnt(0)\n"
    "v_readfirstlane_b32 s59, v116\n"
    "s_mov_b64 exec, 0x3f\n"
    "s_cmp_eq_u32 s59, 7\n"
    "s_cbranch_scc0 2f\n"
    "global_store_dwordx2 v151, v[114:115], s[70:71] sc1\n"
    "2:\n"
    "s_mov_b64 exec, -1\n"
                P1_OPERANDS);
        }
    }
#define ROW01(v) __uint_as_float(__builtin_amdgcn_permlane16_swap(__float_as_uint(v[0]), __float_as_uint(v[1]), false, false)[0])
    STAMP(3); LST(28);

    if (isA) {
        const int h = 4 * xcd + (y >> 3), qb = y & 7, qh = g & 1, kh = g >> 1;
        const unsigned vone = (r == 16 || r == 20) ? 0x3f803f80u : 0u;
        const int t0 = 8 * qb + 4 * qh + 2 * kh;
        const char* ob = ws + WS_O + (unsigned)(t0 * 32 + h) * 1024u; const unsigned fo = WS_FO + ((t0 >> 1) * 32 + h) * 128;
        unsigned bad = FORCE_SLOW;
        {
            const char* k0 = ws + WS_KV + (unsigned)h * 131072u; const char* kv0 = k0 + kh * 65536 + 4096; const char* kvw = kv0 + 6144;
            const char* q0 = ws + WS_Q + (unsigned)(h * 64 + 8 * qb + 4 * qh) * 1024u; const char* qa = q0 + kh * 2048; const char* qc = q0 + (kh ^ 1) * 2048;
            const int fi = lane < 8 ? (32 + h) * 16 + 8 * kh + lane : lane < 16 ? (64 + h) * 16 + 8 * kh + lane - 8 : lane == 16 ? h * 16 + 2 * qb + qh : (32 + h) * 16;
            const unsigned ok = lane * 16, ov = hh * 256 + (r & 15) * 16;
            u32x16 vi = {ok, ov, ok, ov, vone, WS_FQ + fi * 128, L_R + g * 4608 + ok, L_R + (g ^ 2) * 4608 + ok, L_R + g * 4608 + 4096 + lane * 8, L_R + (g ^ 2) * 4608 + 4096 + lane * 8,
                         L_P, L_AP, 1u << (8 * g), ok, ok + 32768, 0u};
            u32x16 si = {LO(k0), HI(k0), LO(qa), HI(qa), LO(qc), HI(qc), LO(q0), HI(q0), LO(kv0), HI(kv0), LO(kvw), HI(kvw), LO(ws), HI(ws), (unsigned)ep, (unsigned)(ep >> 32)};
            u32x4 sj = {LO(ob), HI(ob), fo, 0xffu << (8 * (g ^ 2))};
#pragma unroll
            for (int e = 0; e < 16; ++e) si[e] = __builtin_amdgcn_readfirstlane(si[e]);
#pragma unroll
            for (int e = 0; e < 4; ++e) sj[e] = __builtin_amdgcn_readfirstlane(sj[e]);
            asm volatile(
    "v_mov_b32 v148, v204\n"
    "v_mov_b32 v149, v204\n"
    "v_mov_b32 v150, v204\n"
    "v_mov_b32 v151, v204\n"
    "v_mov_b32 v152, v204\n"
    "v_mov_b32 v153, v204\n"
    "v_mov_b32 v154, v204\n"
    "v_mov_b32 v155, v204\n"
    "v_mov_b32 v160, v204\n"
    "v_mov_b32 v161, v204\n"
    "v_mov_b32 v162, v204\n"
    "v_mov_b32 v163, v204\n"
    "v_mov_b32 v164, v204\n"
    "v_mov_b32 v165, v204\n"
    "v_mov_b32 v166, v204\n"
    "v_mov_b32 v167, v204\n"
    "v_mov_b32 v172, v204\n"
    "v_mov_b32 v173, v204\n"
    "v_mov_b32 v174, v204\n"
    "v_mov_b32 v175, v204\n"
    "v_mov_b32 v176, v204\n"
    "v_mov_b32 v177, v204\n"
    "v_mov_b32 v178, v204\n"
    "v_mov_b32 v179, v204\n"
    "v_mov_b32 v184, v204\n"
    "v_mov_b32 v185, v204\n"
    "v_mov_b32 v186, v204\n"
    "v_mov_b32 v187, v204\n"
    "v_mov_b32 v188, v204\n"
    "v_mov_b32 v189, v204\n"
    "v_mov_b32 v190, v204\n"
    "v_mov_b32 v191, v204\n"
    "v_mov_b32 v192, 0xdddddddd\n"
    "v_mov_b64 v[16:17], 0\n"
    "v_mov_b64 v[18:19], 0\n"
    "v_mov_b64 v[20:21], 0\n"
    "v_mov_b64 v[22:23], 0\n"
    "v_mov_b64 v[24:25], 0\n"
    "v_mov_b64 v[26:27], 0\n"
    "v_mov_b64 v[28:29], 0\n"
    "v_mov_b64 v[30:31], 0\n"
    "v_mov_b64 v[32:33], 0\n"
    "v_mov_b64 v[34:35], 0\n"
    "v_mov_b64 v[36:37], 0\n"
    "v_mov_b64 v[38:39], 0\n"
    "v_mov_b64 v[40:41], 0\n"
    "v_mov_b64 v[42:43], 0\n"
    "v_mov_b64 v[44:45], 0\n"
    "v_mov_b64 v[46:47], 0\n"
    "v_mov_b64 v[48:49], 0\n"
    "v_mov_b64 v[50:51], 0\n"
    "v_mov_b64 v[52:53], 0\n"
    "v_mov_b64 v[54:55], 0\n"
    "v_mov_b64 v[56:57], 0\n"
    "v_mov_b64 v[58:59], 0\n"
    "v_mov_b64 v[60:61], 0\n"
    "v_mov_b64 v[62:63], 0\n"
    "v_mov_b64 v[64:65], 0\n"
    "v_mov_b64 v[66:67], 0\n"
    "v_mov_b64 v[68:69], 0\n"
    "v_mov_b64 v[70:71], 0\n"
    "v_mov_b64 v[72:73], 0\n"
    "v_mov_b64 v[74:75], 0\n"
    "v_mov_b64 v[76:77], 0\n"
    "v_mov_b64 v[78:79], 0\n"
    "v_mov_b64 v[96:97], 0\n"
    "v_mov_b64 v[98:99], 0\n"
    "v_mov_b64 v[100:101], 0\n"
    "v_mov_b64 v[102:103], 0\n"
    "v_mov_b64 v[104:105], 0\n"
    "v_mov_b64 v[106:107], 0\n"
    "v_mov_b64 v[108:109], 0\n"
    "v_mov_b64 v[110:111], 0\n"
    "s_mov_b32 s50, 0\n"
    "s_mov_b32 s52, 0xffff\n"
    "s_mov_b32 s53, 0xffff\n"
    "s_mov_b32 s54, 8192\n"
    "s_mov_b32 s55, 0x40000\n"
    "s_setprio 2\n"
    "4:\n"
    "global_load_dwordx2 v[196:197], v205, s[76:77] sc1\n"
    "s_sub_u32 s55, s55, 1\n"
    "s_cbranch_scc1 5f\n"
    "s_waitcnt vmcnt(0)\n"
    "v_cmp_ne_u64 vcc, s[78:79], v[196:197]\n"
    "s_cbranch_vccnz 4b\n"
    "5:\n"
    "global_load_dwordx4 v[180:183], v200, s[64:65] sc1\n"
    "global_load_dwordx4 v[112:115], v200, s[70:71] sc1\n"
    "global_load_dwordx4 v[144:147], v200, s[72:73] offset:-4096 sc1\n"
    "global_load_dwordx4 v[128:131], v200, s[66:67] offset:0 sc1\n"
    "global_load_dwordx4 v[132:135], v200, s[66:67] offset:1024 sc1\n"
    "global_load_dwordx4 v[136:139], v200, s[68:69] offset:0 sc1\n"
    "global_load_dwordx4 v[140:143], v200, s[68:69] offset:1024 sc1\n"
    "s_mov_b64 exec, s[52:53]\n"
    "global_load_dwordx4 v[148:151], v201, s[72:73] offset:-3072 sc1\n"
    "global_load_dwordx4 v[152:155], v201, s[72:73] offset:-2560 sc1\n"
    "s_mov_b64 exec, -1\n"
    "global_load_dwordx4 v[156:159], v200, s[72:73] offset:-2048 sc1\n"
    "s_mov_b64 exec, s[52:53]\n"
    "global_load_dwordx4 v[160:163], v201, s[72:73] offset:-1024 sc1\n"
    "global_load_dwordx4 v[164:167], v201, s[72:73] offset:-512 sc1\n"
    "s_mov_b64 exec, -1\n"
    "global_load_dwordx4 v[168:171], v200, s[72:73] offset:0 sc1\n"
    "s_mov_b64 exec, s[52:53]\n"
    "global_load_dwordx4 v[172:175], v201, s[72:73] offset:1024 sc1\n"
    "global_load_dwordx4 v[176:179], v201, s[72:73] offset:1536 sc1\n"
    "s_mov_b64 exec, -1\n"
    "s_waitcnt vmcnt(13)\n"
    "v_mfma_f32_32x32x16_bf16 v[80:95], v[180:183], v[112:115], 0\n"
    "s_nop 11\n"
    "v_max3_f32 v193, v80, v81, v82\n"
    "v_max3_f32 v194, v83, v84, v85\n"
    "v_max3_f32 v195, v86, v87, v88\n"
    "v_max3_f32 v193, v193, v89, v90\n"
    "v_max3_f32 v194, v194, v91, v92\n"
    "v_max3_f32 v195, v195, v93, v94\n"
    "v_max3_f32 v193, v193, v194, v95\n"
    "v_max_f32 v193, v193, v195\n"
    "v_mov_b32 v194, v193\n"
    "s_nop 1\n"
    "v_permlane32_swap_b32 v193, v194\n"
    "v_max_f32 v193, v193, v194\n"
    "v_sub_f32 v112, 0, v193\n"
    "v_sub_f32 v113, 0, v193\n"
    "v_mov_b64 v[114:115], v[112:113]\n"
    "v_mov_b64 v[116:117], v[112:113]\n"
    "v_mov_b64 v[118:119], v[112:113]\n"
    "v_mov_b64 v[120:121], v[112:113]\n"
    "v_mov_b64 v[122:123], v[112:113]\n"
    "v_mov_b64 v[124:125], v[112:113]\n"
    "v_mov_b64 v[126:127], v[112:113]\n"
    "s_waitcnt vmcnt(11)\n"
    "s_nop 0\n"
    "v_mfma_f32_32x32x16_bf16 v[80:95], v[144:147], v[128:131], v[112:127]\n"
    "s_waitcnt vmcnt(6)\n"
    "s_nop 9\n"
    "1:\n"
    "v_smfmac_f32_32x32x32_bf16 v[64:79], v[184:187], v[96:103], v192\n"
    "v_exp_f32 v80, v80\n"
    "v_exp_f32 v81, v81\n"
    "v_exp_f32 v82, v82\n"
    "v_smfmac_f32_32x32x32_bf16 v[64:79], v[188:191], v[104:111], v192\n"
    "v_exp_f32 v83, v83\n"
    "v_exp_f32 v84, v84\n"
    "v_mfma_f32_32x32x16_bf16 v[96:111], v[144:147], v[132:135], v[112:127]\n"
    "global_load_dwordx4 v[180:183], v200, s[74:75] offset:-4096 sc1\n"
    "s_mov_b64 exec, s[52:53]\n"
    "global_load_dwordx4 v[184:187], v201, s[74:75] offset:-3072 sc1\n"
    "global_load_dwordx4 v[188:191], v201, s[74:75] offset:-2560 sc1\n"
    "s_mov_b64 exec, -1\n"
    "v_exp_f32 v85, v85\n"
    "v_exp_f32 v86, v86\n"
    "v_exp_f32 v87, v87\n"
    "v_exp_f32 v88, v88\n"
    "v_exp_f32 v89, v89\n"
    "v_exp_f32 v90, v90\n"
    "v_exp_f32 v91, v91\n"
    "v_exp_f32 v92, v92\n"
    "v_exp_f32 v93, v93\n"
    "v_exp_f32 v94, v94\n"
    "v_exp_f32 v95, v95\n"
    "v_smfmac_f32_32x32x32_bf16 v[16:31], v[148:151], v[80:87], v192\n"
    "v_exp_f32 v96, v96\n"
    "v_exp_f32 v97, v97\n"
    "v_exp_f32 v98, v98\n"
    "v_smfmac_f32_32x32x32_bf16 v[16:31], v[152:155], v[88:95], v192\n"
    "v_exp_f32 v99, v99\n"
    "v_exp_f32 v100, v100\n"
    "v_mfma_f32_32x32x16_bf16 v[80:95], v[144:147], v[136:139], v[112:127]\n"
    "v_exp_f32 v101, v101\n"
    "v_exp_f32 v102, v102\n"
    "v_exp_f32 v103, v103\n"
    "v_exp_f32 v104, v104\n"
    "v_exp_f32 v105, v105\n"
    "v_exp_f32 v106, v106\n"
    "v_exp_f32 v107, v107\n"
    "v_exp_f32 v108, v108\n"
    "v_exp_f32 v109, v109\n"
    "v_exp_f32 v110, v110\n"
    "v_exp_f32 v111, v111\n"
    "v_smfmac_f32_32x32x32_bf16 v[32:47], v[148:151], v[96:103], v192\n"
    "v_exp_f32 v80, v80\n"
    "v_exp_f32 v81, v81\n"
    "v_exp_f32 v82, v82\n"
    "v_smfmac_f32_32x32x32_bf16 v[32:47], v[152:155], v[104:111], v192\n"
    "v_exp_f32 v83, v83\n"
    "v_exp_f32 v84, v84\n"
    "v_mfma_f32_32x32x16_bf16 v[96:111], v[144:147], v[140:143], v[112:127]\n"
    "v_exp_f32 v85, v85\n"
    "v_exp_f32 v86, v86\n"
    "v_exp_f32 v87, v87\n"
    "v_exp_f32 v88, v88\n"
    "v_exp_f32 v89, v89\n"
    "v_exp_f32 v90, v90\n"
    "v_exp_f32 v91, v91\n"
    "v_exp_f32 v92, v92\n"
    "v_exp_f32 v93, v93\n"
    "v_exp_f32 v94, v94\n"
    "v_exp_f32 v95, v95\n"
    "v_smfmac_f32_32x32x32_bf16 v[48:63], v[148:151], v[80:87], v192\n"
    "v_exp_f32 v96, v96\n"
    "v_exp_f32 v97, v97\n"
    "v_exp_f32 v98, v98\n"
    "v_smfmac_f32_32x32x32_bf16 v[48:63], v[152:155], v[88:95], v192\n"
    "v_exp_f32 v99, v99\n"
    "v_exp_f32 v100, v100\n"
    "s_waitcnt vmcnt(6)\n"
    "v_mfma_f32_32x32x16_bf16 v[80:95], v[156:159], v[128:131], v[112:127]\n"
    "v_exp_f32 v101, v101\n"
    "v_exp_f32 v102, v102\n"
    "v_exp_f32 v103, v103\n"
    "v_exp_f32 v104, v104\n"
    "v_exp_f32 v105, v105\n"
    "v_exp_f32 v106, v106\n"
    "v_exp_f32 v107, v107\n"
    "v_exp_f32 v108, v108\n"
    "v_exp_f32 v109, v109\n"
    "v_exp_f32 v110, v110\n"
    "v_exp_f32 v111, v111\n"
    "v_smfmac_f32_32x32x32_bf16 v[64:79], v[148:151], v[96:103], v192\n"
    "v_exp_f32 v80, v80\n"
    "v_exp_f32 v81, v81\n"
    "v_exp_f32 v82, v82\n"
    "v_smfmac_f32_32x32x32_bf16 v[64:79], v[152:155], v[104:111], v192\n"
    "v_exp_f32 v83, v83\n"
    "v_exp_f32 v84, v84\n"
    "v_mfma_f32_32x32x16_bf16 v[96:111], v[156:159], v[132:135], v[112:127]\n"
    "global_load_dwordx4 v[144:147], v202, s[74:75] offset:-2048 sc1\n"
    "s_mov_b64 exec, s[52:53]\n"
    "global_load_dwordx4 v[148:151], v203, s[74:75] offset:-1024 sc1\n"
    "global_load_dwordx4 v[152:155], v203, s[74:75] offset:-512 sc1\n"
    "s_mov_b64 exec, -1\n"
    "v_exp_f32 v85, v85\n"
    "v_exp_f32 v86, v86\n"
    "v_exp_f32 v87, v87\n"
    "v_exp_f32 v88, v88\n"
    "v_exp_f32 v89, v89\n"
    "v_exp_f32 v90, v90\n"
    "v_exp_f32 v91, v91\n"
    "v_exp_f32 v92, v92\n"
    "v_exp_f32 v93, v93\n"
    "v_exp_f32 v94, v94\n"
    "v_exp_f32 v95, v95\n"
    "v_smfmac_f32_32x32x32_bf16 v[16:31], v[160:163], v[80:87], v192\n"
    "v_exp_f32 v96, v96\n"
    "v_exp_f32 v97, v97\n"
    "v_exp_f32 v98, v98\n"
    "v_smfmac_f32_32x32x32_bf16 v[16:31], v[164:167], v[88:95], v192\n"
    "v_exp_f32 v99, v99\n"
    "v_exp_f32 v100, v100\n"
    "v_mfma_f32_32x32x16_bf16 v[80:95], v[156:159], v[136:139], v[112:127]\n"
    "v_exp_f32 v101, v101\n"
    "v_exp_f32 v102, v102\n"
    "v_exp_f32 v103, v103\n"
    "v_exp_f32 v104, v104\n"
    "v_exp_f32 v105, v105\n"
    "v_exp_f32 v106, v106\n"
    "v_exp_f32 v107, v107\n"
    "v_exp_f32 v108, v108\n"
    "v_exp_f32 v109, v109\n"
    "v_exp_f32 v110, v110\n"
    "v_exp_f32 v111, v111\n"
    "v_smfmac_f32_32x32x32_bf16 v[32:47], v[160:163], v[96:103], v192\n"
    "v_exp_f32 v80, v80\n"
    "v_exp_f32 v81, v81\n"
    "v_exp_f32 v82, v82\n"
    "v_smfmac_f32_32x32x32_bf16 v[32:47], v[164:167], v[104:111], v192\n"
    "v_exp_f32 v83, v83\n"
    "v_exp_f32 v84, v84\n"
    "v_mfma_f32_32x32x16_bf16 v[96:111], v[156:159], v[140:143], v[112:127]\n"
    "v_exp_f32 v85, v85\n"
    "v_exp_f32 v86, v86\n"
    "v_exp_f32 v87, v87\n"
    "v_exp_f32 v88, v88\n"
    "v_exp_f32 v89, v89\n"
    "v_exp_f32 v90, v90\n"
    "v_exp_f32 v91, v91\n"
    "v_exp_f32 v92, v92\n"
    "v_exp_f32 v93, v93\n"
    "v_exp_f32 v94, v94\n"
    "v_exp_f32 v95, v95\n"
    "v_smfmac_f32_32x32x32_bf16 v[48:63], v[160:163], v[80:87], v192\n"
    "v_exp_f32 v96, v96\n"
    "v_exp_f32 v97, v97\n"
    "v_exp_f32 v98, v98\n"
    "v_smfmac_f32_32x32x32_bf16 v[48:63], v[164:167], v[88:95], v192\n"
    "v_exp_f32 v99, v99\n"
    "v_exp_f32 v100, v100\n"
    "s_waitcnt vmcnt(6)\n"
    "v_mfma_f32_32x32x16_bf16 v[80:95], v[168:171], v[128:131], v[112:127]\n"
    "v_exp_f32 v101, v101\n"
    "v_exp_f32 v102, v102\n"
    "v_exp_f32 v103, v103\n"
    "v_exp_f32 v104, v104\n"
    "v_exp_f32 v105, v105\n"
    "v_exp_f32 v106, v106\n"
    "v_exp_f32 v107, v107\n"
    "v_exp_f32 v108, v108\n"
    "v_exp_f32 v109, v109\n"
    "v_exp_f32 v110, v110\n"
    "v_exp_f32 v111, v111\n"
    "v_smfmac_f32_32x32x32_bf16 v[64:79], v[160:163], v[96:103], v192\n"
    "v_exp_f32 v80, v80\n"
    "v_exp_f32 v81, v81\n"
    "v_exp_f32 v82, v82\n"
    "v_smfmac_f32_32x32x32_bf16 v[64:79], v[164:167], v[104:111], v192\n"
    "v_exp_f32 v83, v83\n"
    "v_exp_f32 v84, v84\n"
    "v_mfma_f32_32x32x16_bf16 v[96:111], v[168:171], v[132:135], v[112:127]\n"
    "global_load_dwordx4 v[156:159], v202, s[74:75] offset:0 sc1\n"
    "s_mov_b64 exec, s[52:53]\n"
    "global_load_dwordx4 v[160:163], v203, s[74:75] offset:1024 sc1\n"
    "global_load_dwordx4 v[164:167], v203, s[74:75] offset:1536 sc1\n"
    "s_mov_b64 exec, -1\n"
    "v_exp_f32 v85, v85\n"
    "v_exp_f32 v86, v86\n"
    "v_exp_f32 v87, v87\n"
    "v_exp_f32 v88, v88\n"
    "v_exp_f32 v89, v89\n"
    "v_exp_f32 v90, v90\n"
    "v_exp_f32 v91, v91\n"
    "v_exp_f32 v92, v92\n"
    "v_exp_f32 v93, v93\n"
    "v_exp_f32 v94, v94\n"
    "v_exp_f32 v95, v95\n"
    "v_smfmac_f32_32x32x32_bf16 v[16:31], v[172:175], v[80:87], v192\n"
    "v_exp_f32 v96, v96\n"
    "v_exp_f32 v97, v97\n"
    "v_exp_f32 v98, v98\n"
    "v_smfmac_f32_32x32x32_bf16 v[16:31], v[176:179], v[88:95], v192\n"
    "v_exp_f32 v99, v99\n"
    "v_exp_f32 v100, v100\n"
    "v_mfma_f32_32x32x16_bf16 v[80:95], v[168:171], v[136:139], v[112:127]\n"
    "v_exp_f32 v101, v101\n"
    "v_exp_f32 v102, v102\n"
    "v_exp_f32 v103, v103\n"
    "v_exp_f32 v104, v104\n"
    "v_exp_f32 v105, v105\n"
    "v_exp_f32 v106, v106\n"
    "v_exp_f32 v107, v107\n"
    "v_exp_f32 v108, v108\n"
    "v_exp_f32 v109, v109\n"
    "v_exp_f32 v110, v110\n"
    "v_exp_f32 v111, v111\n"
    "v_smfmac_f32_32x32x32_bf16 v[32:47], v[172:175], v[96:103], v192\n"
    "v_exp_f32 v80, v80\n"
    "v_exp_f32 v81, v81\n"
    "v_exp_f32 v82, v82\n"
    "v_smfmac_f32_32x32x32_bf16 v[32:47], v[176:179], v[104:111], v192\n"
    "v_exp_f32 v83, v83\n"
    "v_exp_f32 v84, v84\n"
    "v_mfma_f32_32x32x16_bf16 v[96:111], v[168:171], v[140:143], v[112:127]\n"
    "v_exp_f32 v85, v85\n"
    "v_exp_f32 v86, v86\n"
    "v_exp_f32 v87, v87\n"
    "v_exp_f32 v88, v88\n"
    "v_exp_f32 v89, v89\n"
    "v_exp_f32 v90, v90\n"
    "v_exp_f32 v91, v91\n"
    "v_exp_f32 v92, v92\n"
    "v_exp_f32 v93, v93\n"
    "v_exp_f32 v94, v94\n"
    "v_exp_f32 v95, v95\n"
    "v_smfmac_f32_32x32x32_bf16 v[48:63], v[172:175], v[80:87], v192\n"
    "v_exp_f32 v96, v96\n"
    "v_exp_f32 v97, v97\n"
    "v_exp_f32 v98, v98\n"
    "v_smfmac_f32_32x32x32_bf16 v[48:63], v[176:179], v[88:95], v192\n"
    "v_exp_f32 v99, v99\n"
    "v_exp_f32 v100, v100\n"
    "s_waitcnt vmcnt(6)\n"
    "v_mfma_f32_32x32x16_bf16 v[80:95], v[180:183], v[128:131], v[112:127]\n"
    "v_exp_f32 v101, v101\n"
    "v_exp_f32 v102, v102\n"
    "v_exp_f32 v103, v103\n"
    "v_exp_f32 v104, v104\n"
    "v_exp_f32 v105, v105\n"
    "v_exp_f32 v106, v106\n"
    "v_exp_f32 v107, v107\n"
    "v_exp_f32 v108, v108\n"
    "v_exp_f32 v109, v109\n"
    "v_exp_f32 v110, v110\n"
    "v_exp_f32 v111, v111\n"
    "v_smfmac_f32_32x32x32_bf16 v[64:79], v[172:175], v[96:103], v192\n"
    "v_exp_f32 v80, v80\n"
    "v_exp_f32 v81, v81\n"
    "v_exp_f32 v82, v82\n"
    "v_smfmac_f32_32x32x32_bf16 v[64:79], v[176:179], v[104:111], v192\n"
    "v_exp_f32 v83, v83\n"
    "v_exp_f32 v84, v84\n"
    "v_mfma_f32_32x32x16_bf16 v[96:111], v[180:183], v[132:135], v[112:127]\n"
    "global_load_dwordx4 v[168:171], v202, s[74:75] offset:2048 sc1\n"
    "s_mov_b64 exec, s[52:53]\n"
    "global_load_dwordx4 v[172:175], v203, s[74:75] offset:3072 sc1\n"
    "global_load_dwordx4 v[176:179], v203, s[74:75] offset:3584 sc1\n"
    "s_mov_b64 exec, -1\n"
    "v_exp_f32 v85, v85\n"
    "v_exp_f32 v86, v86\n"
    "v_exp_f32 v87, v87\n"
    "v_exp_f32 v88, v88\n"
    "v_exp_f32 v89, v89\n"
    "v_exp_f32 v90, v90\n"
    "v_exp_f32 v91, v91\n"
    "v_exp_f32 v92, v92\n"
    "v_exp_f32 v93, v93\n"
    "v_exp_f32 v94, v94\n"
    "v_exp_f32 v95, v95\n"
    "v_smfmac_f32_32x32x32_bf16 v[16:31], v[184:187], v[80:87], v192\n"
    "v_exp_f32 v96, v96\n"
    "v_exp_f32 v97, v97\n"
    "v_exp_f32 v98, v98\n"
    "v_smfmac_f32_32x32x32_bf16 v[16:31], v[188:191], v[88:95], v192\n"
    "v_exp_f32 v99, v99\n"
    "v_exp_f32 v100, v100\n"
    "v_mfma_f32_32x32x16_bf16 v[80:95], v[180:183], v[136:139], v[112:127]\n"
    "v_exp_f32 v101, v101\n"
    "v_exp_f32 v102, v102\n"
    "v_exp_f32 v103, v103\n"
    "v_exp_f32 v104, v104\n"
    "v_exp_f32 v105, v105\n"
    "v_exp_f32 v106, v106\n"
    "v_exp_f32 v107, v107\n"
    "v_exp_f32 v108, v108\n"
    "v_exp_f32 v109, v109\n"
    "v_exp_f32 v110, v110\n"
    "v_exp_f32 v111, v111\n"
    "v_smfmac_f32_32x32x32_bf16 v[32:47], v[184:187], v[96:103], v192\n"
    "v_exp_f32 v80, v80\n"
    "v_exp_f32 v81, v81\n"
    "v_exp_f32 v82, v82\n"
    "v_smfmac_f32_32x32x32_bf16 v[32:47], v[188:191], v[104:111], v192\n"
    "v_exp_f32 v83, v83\n"
    "v_exp_f32 v84, v84\n"
    "v_mfma_f32_32x32x16_bf16 v[96:111], v[180:183], v[140:143], v[112:127]\n"
    "s_cmp_eq_u32 s50, 6\n"
    "s_cselect_b32 s51, 0, s54\n"
    "v_add_u32 v200, s54, v200\n"
    "v_add_u32 v201, s54, v201\n"
    "v_add_u32 v202, s51, v202\n"
    "v_add_u32 v203, s51, v203\n"
    "s_add_u32 s50, s50, 1\n"
    "s_cmp_lt_u32 s50, 8\n"
    "v_exp_f32 v85, v85\n"
    "v_exp_f32 v86, v86\n"
    "v_exp_f32 v87, v87\n"
    "v_exp_f32 v88, v88\n"
    "v_exp_f32 v89, v89\n"
    "v_exp_f32 v90, v90\n"
    "v_exp_f32 v91, v91\n"
    "v_exp_f32 v92, v92\n"
    "v_exp_f32 v93, v93\n"
    "v_exp_f32 v94, v94\n"
    "v_exp_f32 v95, v95\n"
    "v_smfmac_f32_32x32x32_bf16 v[48:63], v[184:187], v[80:87], v192\n"
    "v_exp_f32 v96, v96\n"
    "v_exp_f32 v97, v97\n"
    "v_exp_f32 v98, v98\n"
    "v_smfmac_f32_32x32x32_bf16 v[48:63], v[188:191], v[88:95], v192\n"
    "v_exp_f32 v99, v99\n"
    "v_exp_f32 v100, v100\n"
    "s_waitcnt vmcnt(6)\n"
    "v_mfma_f32_32x32x16_bf16 v[80:95], v[144:147], v[128:131], v[112:127]\n"
    "v_exp_f32 v101, v101\n"
    "v_exp_f32 v102, v102\n"
    "v_exp_f32 v103, v103\n"
    "v_exp_f32 v104, v104\n"
    "v_exp_f32 v105, v105\n"
    "v_exp_f32 v106, v106\n"
    "v_exp_f32 v107, v107\n"
    "v_exp_f32 v108, v108\n"
    "v_exp_f32 v109, v109\n"
    "v_exp_f32 v110, v110\n"
    "v_exp_f32 v111, v111\n"
    "s_cbranch_scc1 1b\n"
    "ds_read_b32 v195, v210\n"
    "s_nop 0\n"
    "v_smfmac_f32_32x32x32_bf16 v[64:79], v[184:187], v[96:103], v192\n"
    "v_smfmac_f32_32x32x32_bf16 v[64:79], v[188:191], v[104:111], v192\n"
    "s_mov_b32 s55, 0x400000\n"
    "6:\n"
    "s_waitcnt lgkmcnt(0)\n"
    "v_readfirstlane_b32 s51, v195\n"
    "s_sub_u32 s51, s51, 0x01010101\n"
    "s_and_b32 s51, s51, 0x80808080\n"
    "s_cbranch_scc0 7f\n"
    "s_sub_u32 s55, s55, 1\n"
    "s_cbranch_scc1 7f\n"
    "s_sleep 1\n"
    "ds_read_b32 v195, v210\n"
    "s_branch 6b\n"
    "7:\n"
    "ds_write_b128 v206, v[48:51]\n"
    "ds_write_b128 v206, v[52:55] offset:1024\n"
    "s_nop 6\n"
    "ds_write_b128 v206, v[64:67] offset:2048\n"
    "ds_write_b128 v206, v[68:71] offset:3072\n"
    "ds_write2_b32 v208, v56, v72 offset1:1\n"
    "s_mov_b64 exec, 1\n"
    "s_waitcnt lgkmcnt(0)\n"
    "ds_add_u32 v211, v212\n"
    "s_mov_b64 exec, -1\n"
    "s_mov_b32 s55, 0x400000\n"
    "8:\n"
    "ds_read_b32 v195, v211\n"
    "s_sub_u32 s55, s55, 1\n"
    "s_cbranch_scc1 9f\n"
    "s_waitcnt lgkmcnt(0)\n"
    "v_readfirstlane_b32 s51, v195\n"
    "s_and_b32 s51, s51, s83\n"
    "s_cbranch_scc0 8b\n"
    "9:\n"
    "ds_read_b128 v[80:83], v207 offset:0\n"
    "ds_read_b128 v[84:87], v207 offset:1024\n"
    "ds_read_b128 v[88:91], v207 offset:2048\n"
    "ds_read_b128 v[92:95], v207 offset:3072\n"
    "ds_read_b64 v[96:97], v209\n"
    "s_waitcnt lgkmcnt(0)\n"
    "v_add_f32 v24, v24, v96\n"
    "v_add_f32 v40, v40, v97\n"
    "v_pk_add_f32 v[16:17], v[16:17], v[80:81]\n"
    "v_pk_add_f32 v[18:19], v[18:19], v[82:83]\n"
    "v_pk_add_f32 v[20:21], v[20:21], v[84:85]\n"
    "v_pk_add_f32 v[22:23], v[22:23], v[86:87]\n"
    "v_rcp_f32 v112, v24\n"
    "v_rcp_f32 v114, v40\n"
    "v_pk_add_f32 v[32:33], v[32:33], v[88:89]\n"
    "v_pk_add_f32 v[34:35], v[34:35], v[90:91]\n"
    "v_pk_add_f32 v[36:37], v[36:37], v[92:93]\n"
    "v_pk_add_f32 v[38:39], v[38:39], v[94:95]\n"
    "v_pk_mul_f32 v[80:81], v[16:17], v[112:113] op_sel_hi:[1,0]\n"
    "v_pk_mul_f32 v[82:83], v[18:19], v[112:113] op_sel_hi:[1,0]\n"
    "v_pk_mul_f32 v[84:85], v[20:21], v[112:113] op_sel_hi:[1,0]\n"
    "v_pk_mul_f32 v[86:87], v[22:23], v[112:113] op_sel_hi:[1,0]\n"
    "v_cvt_pk_bf16_f32 v128, v80, v81\n"
    "v_cvt_pk_bf16_f32 v129, v82, v83\n"
    "v_cvt_pk_bf16_f32 v130, v84, v85\n"
    "v_cvt_pk_bf16_f32 v131, v86, v87\n"
    "global_store_dwordx4 v213, v[128:131], s[80:81] sc1\n"
    "v_pk_mul_f32 v[88:89], v[32:33], v[114:115] op_sel_hi:[1,0]\n"
    "v_pk_mul_f32 v[90:91], v[34:35], v[114:115] op_sel_hi:[1,0]\n"
    "v_pk_mul_f32 v[92:93], v[36:37], v[114:115] op_sel_hi:[1,0]\n"
    "v_pk_mul_f32 v[94:95], v[38:39], v[114:115] op_sel_hi:[1,0]\n"
    "v_cvt_pk_bf16_f32 v132, v88, v89\n"
    "v_cvt_pk_bf16_f32 v133, v90, v91\n"
    "v_cvt_pk_bf16_f32 v134, v92, v93\n"
    "v_cvt_pk_bf16_f32 v135, v94, v95\n"
    "global_store_dwordx4 v214, v[132:135], s[80:81] sc1\n"
    "v_min_f32 v193, v24, v40\n"
    "v_max_f32 v194, v24, v40\n"
    "v_mov_b32 v195, 0\n"
    "v_fma_f32 v195, v16, 0, v195\n"
    "v_fma_f32 v195, v17, 0, v195\n"
    "v_fma_f32 v195, v18, 0, v195\n"
    "v_fma_f32 v195, v19, 0, v195\n"
    "v_fma_f32 v195, v20, 0, v195\n"
    "v_fma_f32 v195, v21, 0, v195\n"
    "v_fma_f32 v195, v22, 0, v195\n"
    "v_fma_f32 v195, v23, 0, v195\n"
    "v_fma_f32 v195, v32, 0, v195\n"
    "v_fma_f32 v195, v33, 0, v195\n"
    "v_fma_f32 v195, v34, 0, v195\n"
    "v_fma_f32 v195, v35, 0, v195\n"
    "v_fma_f32 v195, v36, 0, v195\n"
    "v_fma_f32 v195, v37, 0, v195\n"
    "v_fma_f32 v195, v38, 0, v195\n"
    "v_fma_f32 v195, v39, 0, v195\n"
    "v_fma_f32 v195, v24, 0, v195\n"
    "v_fma_f32 v195, v40, 0, v195\n"
    "s_mov_b32 s56, 0x2b800000\n"
    "s_mov_b32 s57, 0x5d800000\n"
    "v_cmp_nge_f32 s[58:59], v193, s56\n"
    "v_cmp_nle_f32 s[60:61], v194, s57\n"
    "v_cmp_neq_f32 vcc, 0, v195\n"
    "v_mov_b32 v196, s78\n"
    "v_mov_b32 v197, s79\n"
    "v_mov_b32 v195, s82\n"
    "s_or_b64 s[58:59], s[58:59], s[60:61]\n"
    "s_or_b64 s[58:59], s[58:59], vcc\n"
    "s_cselect_b32 s85, 1, s85\n"
    "s_cmp_lg_u32 s85, 0\n"
    "s_cbranch_scc1 3f\n"
    "s_mov_b64 exec, 1\n"
    "s_waitcnt vmcnt(0)\n"
    "global_store_dwordx2 v195, v[196:197], s[76:77] sc1\n"
    "s_mov_b64 exec, -1\n"
    "3:\n"
    "s_waitcnt vmcnt(0)\n"
                : "+{v[200:215]}"(vi), "+{s85}"(bad) : "{s[64:79]}"(si), "{s[80:83]}"(sj), "{s84}"(g == 0 ? L_DBG + 44 : L_DUM) : "memory", "scc", "vcc", "v16", "v17", "v18", "v19", "v20", "v21", "v22", "v23", "v24", "v25", "v26", "v27", "v28", "v29", "v30", "v31", "v32", "v33", "v34", "v35", "v36", "v37", "v38", "v39", "v40", "v41", "v42", "v43", "v44", "v45", "v46", "v47", "v48", "v49", "v50", "v51", "v52", "v53", "v54", "v55", "v56", "v57", "v58", "v59", "v60", "v61", "v62", "v63", "v64", "v65", "v66", "v67", "v68", "v69", "v70", "v71", "v72", "v73", "v74", "v75", "v76", "v77", "v78", "v79", "v80", "v81", "v82", "v83", "v84", "v85", "v86", "v87", "v88", "v89", "v90", "v91", "v92", "v93", "v94", "v95", "v96", "v97", "v98", "v99", "v100", "v101", "v102", "v103", "v104", "v105", "v106", "v107", "v108", "v109", "v110", "v111", "v112", "v113", "v114", "v115", "v116", "v117", "v118", "v119", "v120", "v121", "v122", "v123", "v124", "v125", "v126", "v127", "v128", "v129", "v130", "v131", "v132", "v133", "v134", "v135", "v136", "v137", "v138", "v139", "v140", "v141", "v142", "v143", "v144", "v145", "v146", "v147", "v148", "v149", "v150", "v151", "v152", "v153", "v154", "v155", "v156", "v157", "v158", "v159", "v160", "v161", "v162", "v163", "v164", "v165", "v166", "v167", "v168", "v169", "v170", "v171", "v172", "v173", "v174", "v175", "v176", "v177", "v178", "v179", "v180", "v181", "v182", "v183", "v184", "v185", "v186", "v187", "v188", "v189", "v190", "v191", "v192", "v193", "v194", "v195", "v196", "v197", "v198", "v199", "s50", "s51", "s52", "s53", "s54", "s55", "s56", "s57", "s58", "s59", "s60", "s61", "s62", "s63");
        }
        STAMP(5); LST(30);
        if (__builtin_amdgcn_readfirstlane(bad)) {
            int ln = lane; OPAQUE(ln);
            poll_flags(ws + WS_FQ, (ln < 16 ? (32 + h) * 16 + ln : (64 + h) * 16 + (ln & 15)) * 128, ep);
            const unsigned vn = ((ln & 31) == 16 || (ln & 31) == 20) ? ONE2 : 0u;
#pragma unroll 1
            for (int t = 0; t < 2; ++t) {
                f32x16 o; attn_slow(ws, h, t0 + t, ln, vn, o);
                const float il = __builtin_amdgcn_rcpf(o[8]);
#pragma unroll
                for (int e = 0; e < 8; ++e) o[e] *= il;
                const u32x4 v = pack8(o, 0); const unsigned vo = ln * 16 + t * 32768; ST16(v, vo, ob, 0);
            }
            WAITV(0);
            if (ln == 0) set_flag(ws + fo, ep);
        }
        if (blockIdx.x == 0 && g == 0) {
#pragma unroll 1
            for (int q = 0; q < 4; ++q) { const int T = 3 * (4 * q + (lane >> 4)); poll_flags(ws + WS_FQ, (((T >> 4) * 32 + (T & 15) * 2) * 16 + (lane & 15)) * 128, ep); }
            if (lane == 0) set_flag(ws + WS_EPOCH, ep);
        }
        STAMP(6); LST(31); LDUMP();
        return;
    }

    __builtin_amdgcn_s_setprio(3);
    {
        u32x16 vi; u32x16 si;
#pragma unroll
        for (int s = 0; s < 4; ++s) vi[s] = L_X + (r >> 3) * 1024 + (r & 7) * 128 + (((2 * s + hh) ^ ((r >> 1) & 7)) << 4);
        const unsigned wa = g < 3 ? mb13 + nb * 1024 + (8 * hh + tq) * 64 + nb * 32 + tp * 8 : mb13 + nb * 512 + (2 * hh + nb) * 128 + tq * 32 + tp * 8;
        vi[4] = wa; vi[5] = wa ^ (g < 3 ? 32 : 128); vi[6] = WS_CS + (((bj * 4 + g) * 16 + bi) * 32 + r) * 16; vi[7] = v1; vi[8] = v3; vi[9] = sel; vi[10] = L_RS + r * 4; vi[11] = WS_CS + (((bj * 4 + g) * 16 + (bi & 8) + 4 * hh) * 32 + r) * 16; vi[12] = lane * 16; vi[13] = (lane >> 3) * 1024 + (y & 7) * 128 + (((lane & 7) ^ (((lane >> 4) & 1) << 2)) << 4); vi[14] = L_P; vi[15] = 1u << (8 * g);
        const char* hb = ws + WS_H + (unsigned)(4 * bi * 112 + 7 * bj + 2 * g) * 1024u;
        si = (u32x16){0u, 0u, 0u, 0u, mb13, g == 0 ? L_DBG + 160 + 44 : L_DUM, ~0u, g < 3 ? ~0u : 0u, LO(hb), HI(hb), LO(w2), HI(w2), L_X, WS_FH + ((bi * 16 + bj) * 4 + g) * 128, (unsigned)ep, (unsigned)(ep >> 32)};
#pragma unroll
        for (int e = 0; e < 16; ++e) si[e] = __builtin_amdgcn_readfirstlane(si[e]);
        u32x4 pf = {LO(w2 + y * 57344), HI(w2 + y * 57344), LO(wo + y * 16384), HI(wo + y * 16384)};
#pragma unroll
        for (int e = 0; e < 4; ++e) pf[e] = __builtin_amdgcn_readfirstlane(pf[e]);
        if (g < 3) asm volatile(
    "v_mov_b32 v200, v217\n"
    "v_mov_b32 v201, v217\n"
    "v_mov_b32 v202, v217\n"
    "v_mov_b32 v203, v217\n"
    "s_mov_b32 s45, 1\n"
    "s_mov_b32 s46, s86\n"
    "s_lshl_b32 s47, s46, 14\n"
    "v_add_u32 v224, s47, v208\n"
    "v_add_u32 v225, s47, v209\n"
    "v_add_u32 v226, s47, v210\n"
    "v_add_u32 v227, s47, v211\n"
    "s_add_u32 s46, s46, 1\n"
    "s_and_b32 s46, s46, 7\n"
    "s_mul_i32 s48, s46, 229376\n"
    "s_lshl_b32 s47, s46, 14\n"
    "s_add_u32 s64, s82, s48\n"
    "s_addc_u32 s65, s83, 0\n"
    "s_add_u32 s66, s84, s48\n"
    "s_addc_u32 s67, s85, 0\n"
    "s_waitcnt vmcnt(6)\n"
    "ds_read_b64_tr_b16 v[176:177], v212 offset:0\n"
    "ds_read_b64_tr_b16 v[178:179], v212 offset:256\n"
    "ds_read_b64_tr_b16 v[180:181], v213 offset:0\n"
    "ds_read_b64_tr_b16 v[182:183], v213 offset:256\n"
    "ds_read_b128 v[144:147], v224 offset:0\n"
    "ds_read_b128 v[148:151], v224 offset:4096\n"
    "ds_read_b128 v[152:155], v224 offset:8192\n"
    "ds_read_b128 v[156:159], v224 offset:12288\n"
    "v_add_u32 v224, s47, v208\n"
    "s_setprio 0\n"
    "s_waitcnt vmcnt(4) lgkmcnt(0)\n"
    "v_mfma_f32_32x32x16_bf16 v[16:31], v[176:179], v[144:147], 0\n"
    "v_mfma_f32_16x16x32_bf16 v[192:195], v[200:203], v[176:179], 0\n"
    "ds_read_b64_tr_b16 v[184:185], v212 offset:2048\n"
    "ds_read_b64_tr_b16 v[186:187], v212 offset:2304\n"
    "ds_read_b64_tr_b16 v[188:189], v213 offset:2048\n"
    "v_mfma_f32_32x32x16_bf16 v[32:47], v[176:179], v[148:151], 0\n"
    "v_mfma_f32_16x16x32_bf16 v[196:199], v[200:203], v[180:183], 0\n"
    "ds_read_b64_tr_b16 v[190:191], v213 offset:2304\n"
    "ds_read_b128 v[160:163], v225 offset:0\n"
    "ds_read_b128 v[164:167], v225 offset:4096\n"
    "v_mfma_f32_32x32x16_bf16 v[48:63], v[176:179], v[152:155], 0\n"
    "ds_read_b128 v[168:171], v225 offset:8192\n"
    "ds_read_b128 v[172:175], v225 offset:12288\n"
    "s_add_u32 m0, s68, 0\n"
    "s_nop 0\n"
    "global_load_lds_dwordx4 v215, s[64:65]\n"
    "v_mfma_f32_32x32x16_bf16 v[64:79], v[176:179], v[156:159], 0\n"
    "s_add_u32 m0, s68, 1024\n"
    "s_nop 0\n"
    "global_load_lds_dwordx4 v216, s[66:67]\n"
    "v_mfma_f32_32x32x16_bf16 v[80:95], v[180:183], v[144:147], 0\n"
    "s_add_u32 s64, s64, 57344\n"
    "s_addc_u32 s65, s65, 0\n"
    "s_add_u32 s66, s66, 57344\n"
    "s_addc_u32 s67, s67, 0\n"
    "v_mfma_f32_32x32x16_bf16 v[96:111], v[180:183], v[148:151], 0\n"
    "v_mfma_f32_32x32x16_bf16 v[112:127], v[180:183], v[152:155], 0\n"
    "v_mfma_f32_32x32x16_bf16 v[128:143], v[180:183], v[156:159], 0\n"
    "s_waitcnt vmcnt(4) lgkmcnt(0)\n"
    "v_mfma_f32_32x32x16_bf16 v[16:31], v[184:187], v[160:163], v[16:31]\n"
    "v_mfma_f32_16x16x32_bf16 v[192:195], v[200:203], v[184:187], v[192:195]\n"
    "ds_read_b64_tr_b16 v[176:177], v212 offset:4096\n"
    "ds_read_b64_tr_b16 v[178:179], v212 offset:4352\n"
    "ds_read_b64_tr_b16 v[180:181], v213 offset:4096\n"
    "v_mfma_f32_32x32x16_bf16 v[32:47], v[184:187], v[164:167], v[32:47]\n"
    "v_mfma_f32_16x16x32_bf16 v[196:199], v[200:203], v[188:191], v[196:199]\n"
    "ds_read_b64_tr_b16 v[182:183], v213 offset:4352\n"
    "ds_read_b128 v[144:147], v226 offset:0\n"
    "ds_read_b128 v[148:151], v226 offset:4096\n"
    "v_mfma_f32_32x32x16_bf16 v[48:63], v[184:187], v[168:171], v[48:63]\n"
    "ds_read_b128 v[152:155], v226 offset:8192\n"
    "ds_read_b128 v[156:159], v226 offset:12288\n"
    "s_add_u32 m0, s68, 2048\n"
    "s_nop 0\n"
    "global_load_lds_dwordx4 v215, s[64:65]\n"
    "v_mfma_f32_32x32x16_bf16 v[64:79], v[184:187], v[172:175], v[64:79]\n"
    "s_add_u32 m0, s68, 3072\n"
    "s_nop 0\n"
    "global_load_lds_dwordx4 v216, s[66:67]\n"
    "v_mfma_f32_32x32x16_bf16 v[80:95], v[188:191], v[160:163], v[80:95]\n"
    "s_add_u32 s64, s64, 57344\n"
    "s_addc_u32 s65, s65, 0\n"
    "s_add_u32 s66, s66, 57344\n"
    "s_addc_u32 s67, s67, 0\n"
    "v_mfma_f32_32x32x16_bf16 v[96:111], v[188:191], v[164:167], v[96:111]\n"
    "v_mfma_f32_32x32x16_bf16 v[112:127], v[188:191], v[168:171], v[112:127]\n"
    "v_mfma_f32_32x32x16_bf16 v[128:143], v[188:191], v[172:175], v[128:143]\n"
    "s_waitcnt vmcnt(4) lgkmcnt(0)\n"
    "v_mfma_f32_32x32x16_bf16 v[16:31], v[176:179], v[144:147], v[16:31]\n"
    "v_mfma_f32_16x16x32_bf16 v[192:195], v[200:203], v[176:179], v[192:195]\n"
    "ds_read_b64_tr_b16 v[184:185], v212 offset:6144\n"
    "ds_read_b64_tr_b16 v[186:187], v212 offset:6400\n"
    "ds_read_b64_tr_b16 v[188:189], v213 offset:6144\n"
    "v_mfma_f32_32x32x16_bf16 v[32:47], v[176:179], v[148:151], v[32:47]\n"
    "v_mfma_f32_16x16x32_bf16 v[196:199], v[200:203], v[180:183], v[196:199]\n"
    "ds_read_b64_tr_b16 v[190:191], v213 offset:6400\n"
    "ds_read_b128 v[160:163], v227 offset:0\n"
    "ds_read_b128 v[164:167], v227 offset:4096\n"
    "v_mfma_f32_32x32x16_bf16 v[48:63], v[176:179], v[152:155], v[48:63]\n"
    "ds_read_b128 v[168:171], v227 offset:8192\n"
    "ds_read_b128 v[172:175], v227 offset:12288\n"
    "s_add_u32 m0, s68, 4096\n"
    "s_nop 0\n"
    "global_load_lds_dwordx4 v215, s[64:65]\n"
    "v_mfma_f32_32x32x16_bf16 v[64:79], v[176:179], v[156:159], v[64:79]\n"
    "s_add_u32 m0, s68, 5120\n"
    "s_nop 0\n"
    "global_load_lds_dwordx4 v216, s[66:67]\n"
    "v_mfma_f32_32x32x16_bf16 v[80:95], v[180:183], v[144:147], v[80:95]\n"
    "s_add_u32 s64, s64, 57344\n"
    "s_addc_u32 s65, s65, 0\n"
    "s_add_u32 s66, s66, 57344\n"
    "s_addc_u32 s67, s67, 0\n"
    "v_mfma_f32_32x32x16_bf16 v[96:111], v[180:183], v[148:151], v[96:111]\n"
    "v_mfma_f32_32x32x16_bf16 v[112:127], v[180:183], v[152:155], v[112:127]\n"
    "v_mfma_f32_32x32x16_bf16 v[128:143], v[180:183], v[156:159], v[128:143]\n"
    "s_waitcnt vmcnt(4) lgkmcnt(0)\n"
    "v_mfma_f32_32x32x16_bf16 v[16:31], v[184:187], v[160:163], v[16:31]\n"
    "v_mfma_f32_16x16x32_bf16 v[192:195], v[200:203], v[184:187], v[192:195]\n"
    "ds_read_b64_tr_b16 v[176:177], v212 offset:0\n"
    "ds_read_b64_tr_b16 v[178:179], v212 offset:256\n"
    "ds_read_b64_tr_b16 v[180:181], v213 offset:0\n"
    "v_mfma_f32_32x32x16_bf16 v[32:47], v[184:187], v[164:167], v[32:47]\n"
    "v_mfma_f32_16x16x32_bf16 v[196:199], v[200:203], v[188:191], v[196:199]\n"
    "ds_read_b64_tr_b16 v[182:183], v213 offset:256\n"
    "ds_read_b128 v[144:147], v224 offset:0\n"
    "ds_read_b128 v[148:151], v224 offset:4096\n"
    "v_mfma_f32_32x32x16_bf16 v[48:63], v[184:187], v[168:171], v[48:63]\n"
    "ds_read_b128 v[152:155], v224 offset:8192\n"
    "ds_read_b128 v[156:159], v224 offset:12288\n"
    "s_add_u32 m0, s68, 6144\n"
    "s_nop 0\n"
    "global_load_lds_dwordx4 v215, s[64:65]\n"
    "v_mfma_f32_32x32x16_bf16 v[64:79], v[184:187], v[172:175], v[64:79]\n"
    "s_add_u32 m0, s68, 7168\n"
    "s_nop 0\n"
    "global_load_lds_dwordx4 v216, s[66:67]\n"
    "v_mfma_f32_32x32x16_bf16 v[80:95], v[188:191], v[160:163], v[80:95]\n"
    "v_mfma_f32_32x32x16_bf16 v[96:111], v[188:191], v[164:167], v[96:111]\n"
    "s_setprio 3\n"
    "v_add_u32 v225, s47, v209\n"
    "v_add_u32 v226, s47, v210\n"
    "v_add_u32 v227, s47, v211\n"
    "s_add_u32 s46, s46, 1\n"
    "s_and_b32 s46, s46, 7\n"
    "s_mul_i32 s48, s46, 229376\n"
    "s_lshl_b32 s47, s46, 14\n"
    "s_add_u32 s64, s82, s48\n"
    "s_addc_u32 s65, s83, 0\n"
    "s_add_u32 s66, s84, s48\n"
    "s_addc_u32 s67, s85, 0\n"
    "v_add_u32 v224, s47, v208\n"
    "v_permlane16_swap_b32 v192, v193\n"
    "v_permlane16_swap_b32 v196, v197\n"
    "v_mov_b32 v194, s78\n"
    "v_mov_b32 v195, s79\n"
    "v_mov_b32 v193, v196\n"
    "s_mov_b64 exec, 0xffffffff\n"
    "global_store_dwordx4 v214, v[192:195], s[80:81] sc1\n"
    "s_mov_b64 exec, -1\n"
    "s_setprio 0\n"
    "v_mfma_f32_32x32x16_bf16 v[112:127], v[188:191], v[168:171], v[112:127]\n"
    "v_mfma_f32_32x32x16_bf16 v[128:143], v[188:191], v[172:175], v[128:143]\n"
    "1:\n"
    "s_waitcnt vmcnt(4) lgkmcnt(0)\n"
    "v_mfma_f32_32x32x16_bf16 v[16:31], v[176:179], v[144:147], v[16:31]\n"
    "ds_read_b64_tr_b16 v[184:185], v212 offset:2048\n"
    "ds_read_b64_tr_b16 v[186:187], v212 offset:2304\n"
    "ds_read_b64_tr_b16 v[188:189], v213 offset:2048\n"
    "v_mfma_f32_32x32x16_bf16 v[32:47], v[176:179], v[148:151], v[32:47]\n"
    "ds_read_b64_tr_b16 v[190:191], v213 offset:2304\n"
    "ds_read_b128 v[160:163], v225 offset:0\n"
    "ds_read_b128 v[164:167], v225 offset:4096\n"
    "v_mfma_f32_32x32x16_bf16 v[48:63], v[176:179], v[152:155], v[48:63]\n"
    "ds_read_b128 v[168:171], v225 offset:8192\n"
    "ds_read_b128 v[172:175], v225 offset:12288\n"
    "s_add_u32 m0, s68, 0\n"
    "s_nop 0\n"
    "global_load_lds_dwordx4 v215, s[64:65]\n"
    "v_mfma_f32_32x32x16_bf16 v[64:79], v[176:179], v[156:159], v[64:79]\n"
    "s_add_u32 m0, s68, 1024\n"
    "s_nop 0\n"
    "global_load_lds_dwordx4 v216, s[66:67]\n"
    "v_mfma_f32_32x32x16_bf16 v[80:95], v[180:183], v[144:147], v[80:95]\n"
    "s_add_u32 s64, s64, 57344\n"
    "s_addc_u32 s65, s65, 0\n"
    "s_add_u32 s66, s66, 57344\n"
    "s_addc_u32 s67, s67, 0\n"
    "v_mfma_f32_32x32x16_bf16 v[96:111], v[180:183], v[148:151], v[96:111]\n"
    "s_cmp_eq_u32 s45, 7\n"
    "s_cbranch_scc0 2f\n"
    "global_load_dwordx4 v[228:231], v219, s[80:81] offset:0 sc1\n"
    "global_load_dwordx4 v[232:235], v219, s[80:81] offset:512 sc1\n"
    "global_load_dwordx4 v[236:239], v219, s[80:81] offset:1024 sc1\n"
    "global_load_dwordx4 v[240:243], v219, s[80:81] offset:1536 sc1\n"
    "2:\n"
    "v_mfma_f32_32x32x16_bf16 v[112:127], v[180:183], v[152:155], v[112:127]\n"
    "v_mfma_f32_32x32x16_bf16 v[128:143], v[180:183], v[156:159], v[128:143]\n"
    "s_waitcnt vmcnt(4) lgkmcnt(0)\n"
    "v_mfma_f32_32x32x16_bf16 v[16:31], v[184:187], v[160:163], v[16:31]\n"
    "ds_read_b64_tr_b16 v[176:177], v212 offset:4096\n"
    "ds_read_b64_tr_b16 v[178:179], v212 offset:4352\n"
    "ds_read_b64_tr_b16 v[180:181], v213 offset:4096\n"
    "v_mfma_f32_32x32x16_bf16 v[32:47], v[184:187], v[164:167], v[32:47]\n"
    "ds_read_b64_tr_b16 v[182:183], v213 offset:4352\n"
    "ds_read_b128 v[144:147], v226 offset:0\n"
    "ds_read_b128 v[148:151], v226 offset:4096\n"
    "v_mfma_f32_32x32x16_bf16 v[48:63], v[184:187], v[168:171], v[48:63]\n"
    "ds_read_b128 v[152:155], v226 offset:8192\n"
    "ds_read_b128 v[156:159], v226 offset:12288\n"
    "s_add_u32 m0, s68, 2048\n"
    "s_nop 0\n"
    "global_load_lds_dwordx4 v215, s[64:65]\n"
    "v_mfma_f32_32x32x16_bf16 v[64:79], v[184:187], v[172:175], v[64:79]\n"
    "s_add_u32 m0, s68, 3072\n"
    "s_nop 0\n"
    "global_load_lds_dwordx4 v216, s[66:67]\n"
    "v_mfma_f32_32x32x16_bf16 v[80:95], v[188:191], v[160:163], v[80:95]\n"
    "s_add_u32 s64, s64, 57344\n"
    "s_addc_u32 s65, s65, 0\n"
    "s_add_u32 s66, s66, 57344\n"
    "s_addc_u32 s67, s67, 0\n"
    "v_mfma_f32_32x32x16_bf16 v[96:111], v[188:191], v[164:167], v[96:111]\n"
    "v_mfma_f32_32x32x16_bf16 v[112:127], v[188:191], v[168:171], v[112:127]\n"
    "v_mfma_f32_32x32x16_bf16 v[128:143], v[188:191], v[172:175], v[128:143]\n"
    "s_waitcnt vmcnt(4) lgkmcnt(0)\n"
    "v_mfma_f32_32x32x16_bf16 v[16:31], v[176:179], v[144:147], v[16:31]\n"
    "ds_read_b64_tr_b16 v[184:185], v212 offset:6144\n"
    "ds_read_b64_tr_b16 v[186:187], v212 offset:6400\n"
    "ds_read_b64_tr_b16 v[188:189], v213 offset:6144\n"
    "v_mfma_f32_32x32x16_bf16 v[32:47], v[176:179], v[148:151], v[32:47]\n"
    "ds_read_b64_tr_b16 v[190:191], v213 offset:6400\n"
    "ds_read_b128 v[160:163], v227 offset:0\n"
    "ds_read_b128 v[164:167], v227 offset:4096\n"
    "v_mfma_f32_32x32x16_bf16 v[48:63], v[176:179], v[152:155], v[48:63]\n"
    "ds_read_b128 v[168:171], v227 offset:8192\n"
    "ds_read_b128 v[172:175], v227 offset:12288\n"
    "s_add_u32 m0, s68, 4096\n"
    "s_nop 0\n"
    "global_load_lds_dwordx4 v215, s[64:65]\n"
    "v_mfma_f32_32x32x16_bf16 v[64:79], v[176:179], v[156:159], v[64:79]\n"
    "s_add_u32 m0, s68, 5120\n"
    "s_nop 0\n"
    "global_load_lds_dwordx4 v216, s[66:67]\n"
    "v_mfma_f32_32x32x16_bf16 v[80:95], v[180:183], v[144:147], v[80:95]\n"
    "s_add_u32 s64, s64, 57344\n"
    "s_addc_u32 s65, s65, 0\n"
    "s_add_u32 s66, s66, 57344\n"
    "s_addc_u32 s67, s67, 0\n"
    "v_mfma_f32_32x32x16_bf16 v[96:111], v[180:183], v[148:151], v[96:111]\n"
    "v_mfma_f32_32x32x16_bf16 v[112:127], v[180:183], v[152:155], v[112:127]\n"
    "v_mfma_f32_32x32x16_bf16 v[128:143], v[180:183], v[156:159], v[128:143]\n"
    "s_waitcnt vmcnt(4) lgkmcnt(0)\n"
    "v_mfma_f32_32x32x16_bf16 v[16:31], v[184:187], v[160:163], v[16:31]\n"
    "ds_read_b64_tr_b16 v[176:177], v212 offset:0\n"
    "ds_read_b64_tr_b16 v[178:179], v212 offset:256\n"
    "ds_read_b64_tr_b16 v[180:181], v213 offset:0\n"
    "v_mfma_f32_32x32x16_bf16 v[32:47], v[184:187], v[164:167], v[32:47]\n"
    "ds_read_b64_tr_b16 v[182:183], v213 offset:256\n"
    "ds_read_b128 v[144:147], v224 offset:0\n"
    "ds_read_b128 v[148:151], v224 offset:4096\n"
    "v_mfma_f32_32x32x16_bf16 v[48:63], v[184:187], v[168:171], v[48:63]\n"
    "ds_read_b128 v[152:155], v224 offset:8192\n"
    "ds_read_b128 v[156:159], v224 offset:12288\n"
    "s_add_u32 m0, s68, 6144\n"
    "s_nop 0\n"
    "global_load_lds_dwordx4 v215, s[64:65]\n"
    "v_mfma_f32_32x32x16_bf16 v[64:79], v[184:187], v[172:175], v[64:79]\n"
    "s_add_u32 m0, s68, 7168\n"
    "s_nop 0\n"
    "global_load_lds_dwordx4 v216, s[66:67]\n"
    "v_mfma_f32_32x32x16_bf16 v[80:95], v[188:191], v[160:163], v[80:95]\n"
    "v_mfma_f32_32x32x16_bf16 v[96:111], v[188:191], v[164:167], v[96:111]\n"
    "s_setprio 3\n"
    "v_add_u32 v225, s47, v209\n"
    "v_add_u32 v226, s47, v210\n"
    "v_add_u32 v227, s47, v211\n"
    "s_add_u32 s46, s46, 1\n"
    "s_and_b32 s46, s46, 7\n"
    "s_mul_i32 s48, s46, 229376\n"
    "s_lshl_b32 s47, s46, 14\n"
    "s_add_u32 s64, s82, s48\n"
    "s_addc_u32 s65, s83, 0\n"
    "s_add_u32 s66, s84, s48\n"
    "s_addc_u32 s67, s85, 0\n"
    "v_add_u32 v224, s47, v208\n"
    "s_setprio 0\n"
    "s_add_u32 s45, s45, 1\n"
    "s_cmp_lt_u32 s45, 8\n"
    "v_mfma_f32_32x32x16_bf16 v[112:127], v[188:191], v[168:171], v[112:127]\n"
    "v_mfma_f32_32x32x16_bf16 v[128:143], v[188:191], v[172:175], v[128:143]\n"
    "s_cbranch_scc1 1b\n"
    "s_setprio 3\n"
    "s_waitcnt vmcnt(0) lgkmcnt(0)\n"
    "s_mov_b64 exec, 1\n"
    "ds_add_u32 v222, v223\n"
    "s_mov_b64 exec, -1\n"
    "ds_read_b32 v168, v218 offset:0\n"
    "ds_read_b32 v152, v218 offset:512\n"
    "ds_read_b32 v172, v218 offset:128\n"
    "ds_read_b32 v156, v218 offset:640\n"
    "ds_read_b32 v176, v218 offset:256\n"
    "ds_read_b32 v160, v218 offset:768\n"
    "ds_read_b32 v180, v218 offset:384\n"
    "ds_read_b32 v164, v218 offset:896\n"
    "s_mov_b32 s45, 0x40000\n"
    "3:\n"
    "v_cmp_ne_u64 vcc, s[78:79], v[230:231]\n"
    "v_cmp_ne_u64 s[46:47], s[78:79], v[234:235]\n"
    "s_nop 0\n"
    "s_or_b64 vcc, vcc, s[46:47]\n"
    "v_cmp_ne_u64 s[46:47], s[78:79], v[238:239]\n"
    "s_nop 0\n"
    "s_or_b64 vcc, vcc, s[46:47]\n"
    "v_cmp_ne_u64 s[46:47], s[78:79], v[242:243]\n"
    "s_nop 0\n"
    "s_or_b64 vcc, vcc, s[46:47]\n"
    "s_cbranch_scc0 4f\n"
    "s_sub_u32 s45, s45, 1\n"
    "s_cbranch_scc1 4f\n"
    "global_load_dwordx4 v[228:231], v219, s[80:81] offset:0 sc1\n"
    "global_load_dwordx4 v[232:235], v219, s[80:81] offset:512 sc1\n"
    "global_load_dwordx4 v[236:239], v219, s[80:81] offset:1024 sc1\n"
    "global_load_dwordx4 v[240:243], v219, s[80:81] offset:1536 sc1\n"
    "s_waitcnt vmcnt(0)\n"
    "s_branch 3b\n"
    "4:\n"
    "v_pk_add_f32 v[228:229], v[228:229], v[232:233]\n"
    "v_pk_add_f32 v[236:237], v[236:237], v[240:241]\n"
    "v_mov_b64 v[144:145], 0\n"
    "v_mov_b64 v[146:147], 0\n"
    "v_mov_b64 v[148:149], 0\n"
    "v_mov_b64 v[150:151], 0\n"
    "v_pk_add_f32 v[228:229], v[228:229], v[236:237]\n"
    "v_mov_b32 v153, 0\n"
    "v_mov_b64 v[154:155], 0\n"
    "v_mov_b32 v157, 0\n"
    "v_mov_b64 v[158:159], 0\n"
    "v_mov_b32 v161, 0\n"
    "v_mov_b64 v[162:163], 0\n"
    "v_mov_b32 v165, 0\n"
    "v_mov_b64 v[166:167], 0\n"
    "v_mov_b64 v[230:231], v[228:229]\n"
    "s_nop 1\n"
    "v_permlane32_swap_b32 v228, v230\n"
    "v_permlane32_swap_b32 v229, v231\n"
    "v_pk_add_f32 v[228:229], v[228:229], v[230:231]\n"
    "s_mov_b64 exec, 0xffffffff\n"
    "v_cvt_pk_bf16_f32 v184, v228, v228\n"
    "v_lshlrev_b32 v184, 16, v184\n"
    "v_sub_f32 v184, v228, v184\n"
    "v_cvt_pk_bf16_f32 v144, v228, v184\n"
    "v_cvt_pk_bf16_f32 v185, v229, v229\n"
    "v_lshlrev_b32 v185, 16, v185\n"
    "v_sub_f32 v185, v229, v185\n"
    "v_cvt_pk_bf16_f32 v148, v229, v185\n"
    "s_mov_b64 exec, -1\n"
    "s_waitcnt lgkmcnt(0)\n"
    "v_mul_f32 v170, v168, v168\n"
    "v_mul_f32 v174, v172, v172\n"
    "v_mul_f32 v178, v176, v176\n"
    "v_mul_f32 v182, v180, v180\n"
    "v_mfma_f32_32x32x16_bf16 v[16:31], v[144:147], v[152:155], v[16:31]\n"
    "v_rcp_f32 v170, v170\n"
    "v_rcp_f32 v174, v174\n"
    "v_rcp_f32 v178, v178\n"
    "v_rcp_f32 v182, v182\n"
    "v_mfma_f32_32x32x16_bf16 v[80:95], v[148:151], v[152:155], v[80:95]\n"
    "v_mul_f32 v168, 0xbfb8aa3b, v168\n"
    "v_mul_f32 v172, 0xbfb8aa3b, v172\n"
    "v_mul_f32 v176, 0xbfb8aa3b, v176\n"
    "v_mul_f32 v180, 0xbfb8aa3b, v180\n"
    "s_nop 2\n"
    "v_add_u32 v204, 0x2000, v221\n"
    "v_pk_mul_f32 v[184:185], v[16:17], v[168:169] op_sel_hi:[1,0]\n"
    "v_pk_mul_f32 v[186:187], v[18:19], v[168:169] op_sel_hi:[1,0]\n"
    "v_pk_mul_f32 v[188:189], v[20:21], v[168:169] op_sel_hi:[1,0]\n"
    "v_pk_mul_f32 v[190:191], v[22:23], v[168:169] op_sel_hi:[1,0]\n"
    "v_mfma_f32_32x32x16_bf16 v[32:47], v[144:147], v[156:159], v[32:47]\n"
    "v_exp_f32 v184, v184\n"
    "v_exp_f32 v185, v185\n"
    "v_exp_f32 v186, v186\n"
    "v_exp_f32 v187, v187\n"
    "v_exp_f32 v188, v188\n"
    "v_exp_f32 v189, v189\n"
    "v_exp_f32 v190, v190\n"
    "v_exp_f32 v191, v191\n"
    "v_pk_mul_f32 v[16:17], v[16:17], v[24:25]\n"
    "v_pk_mul_f32 v[18:19], v[18:19], v[26:27]\n"
    "v_pk_mul_f32 v[20:21], v[20:21], v[28:29]\n"
    "v_pk_mul_f32 v[22:23], v[22:23], v[30:31]\n"
    "v_pk_fma_f32 v[184:185], v[184:185], v[170:171], v[170:171] op_sel_hi:[1,0,0]\n"
    "v_pk_fma_f32 v[186:187], v[186:187], v[170:171], v[170:171] op_sel_hi:[1,0,0]\n"
    "v_pk_fma_f32 v[188:189], v[188:189], v[170:171], v[170:171] op_sel_hi:[1,0,0]\n"
    "v_pk_fma_f32 v[190:191], v[190:191], v[170:171], v[170:171] op_sel_hi:[1,0,0]\n"
    "v_rcp_f32 v184, v184\n"
    "v_rcp_f32 v185, v185\n"
    "v_rcp_f32 v186, v186\n"
    "v_rcp_f32 v187, v187\n"
    "v_rcp_f32 v188, v188\n"
    "v_rcp_f32 v189, v189\n"
    "v_rcp_f32 v190, v190\n"
    "v_rcp_f32 v191, v191\n"
    "v_pk_mul_f32 v[16:17], v[16:17], v[184:185]\n"
    "v_pk_mul_f32 v[18:19], v[18:19], v[186:187]\n"
    "v_pk_mul_f32 v[20:21], v[20:21], v[188:189]\n"
    "v_pk_mul_f32 v[22:23], v[22:23], v[190:191]\n"
    "v_cvt_pk_bf16_f32 v192, v16, v17\n"
    "v_cvt_pk_bf16_f32 v193, v18, v19\n"
    "v_cvt_pk_bf16_f32 v194, v20, v21\n"
    "v_cvt_pk_bf16_f32 v195, v22, v23\n"
    "global_store_dwordx4 v220, v[192:195], s[72:73] offset:0 sc1\n"
    "v_pk_mul_f32 v[184:185], v[80:81], v[168:169] op_sel_hi:[1,0]\n"
    "v_pk_mul_f32 v[186:187], v[82:83], v[168:169] op_sel_hi:[1,0]\n"
    "v_pk_mul_f32 v[188:189], v[84:85], v[168:169] op_sel_hi:[1,0]\n"
    "v_pk_mul_f32 v[190:191], v[86:87], v[168:169] op_sel_hi:[1,0]\n"
    "v_mfma_f32_32x32x16_bf16 v[96:111], v[148:151], v[156:159], v[96:111]\n"
    "v_exp_f32 v184, v184\n"
    "v_exp_f32 v185, v185\n"
    "v_exp_f32 v186, v186\n"
    "v_exp_f32 v187, v187\n"
    "v_exp_f32 v188, v188\n"
    "v_exp_f32 v189, v189\n"
    "v_exp_f32 v190, v190\n"
    "v_exp_f32 v191, v191\n"
    "v_pk_mul_f32 v[80:81], v[80:81], v[88:89]\n"
    "v_pk_mul_f32 v[82:83], v[82:83], v[90:91]\n"
    "v_pk_mul_f32 v[84:85], v[84:85], v[92:93]\n"
    "v_pk_mul_f32 v[86:87], v[86:87], v[94:95]\n"
    "v_pk_fma_f32 v[184:185], v[184:185], v[170:171], v[170:171] op_sel_hi:[1,0,0]\n"
    "v_pk_fma_f32 v[186:187], v[186:187], v[170:171], v[170:171] op_sel_hi:[1,0,0]\n"
    "v_pk_fma_f32 v[188:189], v[188:189], v[170:171], v[170:171] op_sel_hi:[1,0,0]\n"
    "v_pk_fma_f32 v[190:191], v[190:191], v[170:171], v[170:171] op_sel_hi:[1,0,0]\n"
    "v_rcp_f32 v184, v184\n"
    "v_rcp_f32 v185, v185\n"
    "v_rcp_f32 v186, v186\n"
    "v_rcp_f32 v187, v187\n"
    "v_rcp_f32 v188, v188\n"
    "v_rcp_f32 v189, v189\n"
    "v_rcp_f32 v190, v190\n"
    "v_rcp_f32 v191, v191\n"
    "v_pk_mul_f32 v[80:81], v[80:81], v[184:185]\n"
    "v_pk_mul_f32 v[82:83], v[82:83], v[186:187]\n"
    "v_pk_mul_f32 v[84:85], v[84:85], v[188:189]\n"
    "v_pk_mul_f32 v[86:87], v[86:87], v[190:191]\n"
    "v_cvt_pk_bf16_f32 v196, v80, v81\n"
    "v_cvt_pk_bf16_f32 v197, v82, v83\n"
    "v_cvt_pk_bf16_f32 v198, v84, v85\n"
    "v_cvt_pk_bf16_f32 v199, v86, v87\n"
    "global_store_dwordx4 v220, v[196:199], s[72:73] offset:1024 sc1\n"
    "v_pk_mul_f32 v[184:185], v[32:33], v[172:173] op_sel_hi:[1,0]\n"
    "v_pk_mul_f32 v[186:187], v[34:35], v[172:173] op_sel_hi:[1,0]\n"
    "v_pk_mul_f32 v[188:189], v[36:37], v[172:173] op_sel_hi:[1,0]\n"
    "v_pk_mul_f32 v[190:191], v[38:39], v[172:173] op_sel_hi:[1,0]\n"
    "v_mfma_f32_32x32x16_bf16 v[48:63], v[144:147], v[160:163], v[48:63]\n"
    "v_exp_f32 v184, v184\n"
    "v_exp_f32 v185, v185\n"
    "v_exp_f32 v186, v186\n"
    "v_exp_f32 v187, v187\n"
    "v_exp_f32 v188, v188\n"
    "v_exp_f32 v189, v189\n"
    "v_exp_f32 v190, v190\n"
    "v_exp_f32 v191, v191\n"
    "v_pk_mul_f32 v[32:33], v[32:33], v[40:41]\n"
    "v_pk_mul_f32 v[34:35], v[34:35], v[42:43]\n"
    "v_pk_mul_f32 v[36:37], v[36:37], v[44:45]\n"
    "v_pk_mul_f32 v[38:39], v[38:39], v[46:47]\n"
    "v_pk_fma_f32 v[184:185], v[184:185], v[174:175], v[174:175] op_sel_hi:[1,0,0]\n"
    "v_pk_fma_f32 v[186:187], v[186:187], v[174:175], v[174:175] op_sel_hi:[1,0,0]\n"
    "v_pk_fma_f32 v[188:189], v[188:189], v[174:175], v[174:175] op_sel_hi:[1,0,0]\n"
    "v_pk_fma_f32 v[190:191], v[190:191], v[174:175], v[174:175] op_sel_hi:[1,0,0]\n"
    "v_rcp_f32 v184, v184\n"
    "v_rcp_f32 v185, v185\n"
    "v_rcp_f32 v186, v186\n"
    "v_rcp_f32 v187, v187\n"
    "v_rcp_f32 v188, v188\n"
    "v_rcp_f32 v189, v189\n"
    "v_rcp_f32 v190, v190\n"
    "v_rcp_f32 v191, v191\n"
    "s_add_u32 s72, s72, 114688\n"
    "s_addc_u32 s73, s73, 0\n"
    "v_pk_mul_f32 v[32:33], v[32:33], v[184:185]\n"
    "v_pk_mul_f32 v[34:35], v[34:35], v[186:187]\n"
    "v_pk_mul_f32 v[36:37], v[36:37], v[188:189]\n"
    "v_pk_mul_f32 v[38:39], v[38:39], v[190:191]\n"
    "v_cvt_pk_bf16_f32 v192, v32, v33\n"
    "v_cvt_pk_bf16_f32 v193, v34, v35\n"
    "v_cvt_pk_bf16_f32 v194, v36, v37\n"
    "v_cvt_pk_bf16_f32 v195, v38, v39\n"
    "global_store_dwordx4 v220, v[192:195], s[72:73] offset:0 sc1\n"
    "v_pk_mul_f32 v[184:185], v[96:97], v[172:173] op_sel_hi:[1,0]\n"
    "v_pk_mul_f32 v[186:187], v[98:99], v[172:173] op_sel_hi:[1,0]\n"
    "v_pk_mul_f32 v[188:189], v[100:101], v[172:173] op_sel_hi:[1,0]\n"
    "v_pk_mul_f32 v[190:191], v[102:103], v[172:173] op_sel_hi:[1,0]\n"
    "v_mfma_f32_32x32x16_bf16 v[112:127], v[148:151], v[160:163], v[112:127]\n"
    "v_exp_f32 v184, v184\n"
    "v_exp_f32 v185, v185\n"
    "v_exp_f32 v186, v186\n"
    "v_exp_f32 v187, v187\n"
    "v_exp_f32 v188, v188\n"
    "v_exp_f32 v189, v189\n"
    "v_exp_f32 v190, v190\n"
    "v_exp_f32 v191, v191\n"
    "v_pk_mul_f32 v[96:97], v[96:97], v[104:105]\n"
    "v_pk_mul_f32 v[98:99], v[98:99], v[106:107]\n"
    "v_pk_mul_f32 v[100:101], v[100:101], v[108:109]\n"
    "v_pk_mul_f32 v[102:103], v[102:103], v[110:111]\n"
    "v_pk_fma_f32 v[184:185], v[184:185], v[174:175], v[174:175] op_sel_hi:[1,0,0]\n"
    "v_pk_fma_f32 v[186:187], v[186:187], v[174:175], v[174:175] op_sel_hi:[1,0,0]\n"
    "v_pk_fma_f32 v[188:189], v[188:189], v[174:175], v[174:175] op_sel_hi:[1,0,0]\n"
    "v_pk_fma_f32 v[190:191], v[190:191], v[174:175], v[174:175] op_sel_hi:[1,0,0]\n"
    "v_rcp_f32 v184, v184\n"
    "v_rcp_f32 v185, v185\n"
    "v_rcp_f32 v186, v186\n"
    "v_rcp_f32 v187, v187\n"
    "v_rcp_f32 v188, v188\n"
    "v_rcp_f32 v189, v189\n"
    "v_rcp_f32 v190, v190\n"
    "v_rcp_f32 v191, v191\n"
    "v_pk_mul_f32 v[96:97], v[96:97], v[184:185]\n"
    "v_pk_mul_f32 v[98:99], v[98:99], v[186:187]\n"
    "v_pk_mul_f32 v[100:101], v[100:101], v[188:189]\n"
    "v_pk_mul_f32 v[102:103], v[102:103], v[190:191]\n"
    "v_cvt_pk_bf16_f32 v196, v96, v97\n"
    "v_cvt_pk_bf16_f32 v197, v98, v99\n"
    "v_cvt_pk_bf16_f32 v198, v100, v101\n"
    "v_cvt_pk_bf16_f32 v199, v102, v103\n"
    "global_store_dwordx4 v220, v[196:199], s[72:73] offset:1024 sc1\n"
    "v_pk_mul_f32 v[184:185], v[48:49], v[176:177] op_sel_hi:[1,0]\n"
    "v_pk_mul_f32 v[186:187], v[50:51], v[176:177] op_sel_hi:[1,0]\n"
    "v_pk_mul_f32 v[188:189], v[52:53], v[176:177] op_sel_hi:[1,0]\n"
    "v_pk_mul_f32 v[190:191], v[54:55], v[176:177] op_sel_hi:[1,0]\n"
    "v_mfma_f32_32x32x16_bf16 v[64:79], v[144:147], v[164:167], v[64:79]\n"
    "v_exp_f32 v184, v184\n"
    "v_exp_f32 v185, v185\n"
    "v_exp_f32 v186, v186\n"
    "v_exp_f32 v187, v187\n"
    "v_exp_f32 v188, v188\n"
    "v_exp_f32 v189, v189\n"
    "v_exp_f32 v190, v190\n"
    "v_exp_f32 v191, v191\n"
    "v_pk_mul_f32 v[48:49], v[48:49], v[56:57]\n"
    "v_pk_mul_f32 v[50:51], v[50:51], v[58:59]\n"
    "v_pk_mul_f32 v[52:53], v[52:53], v[60:61]\n"
    "v_pk_mul_f32 v[54:55], v[54:55], v[62:63]\n"
    "v_pk_fma_f32 v[184:185], v[184:185], v[178:179], v[178:179] op_sel_hi:[1,0,0]\n"
    "v_pk_fma_f32 v[186:187], v[186:187], v[178:179], v[178:179] op_sel_hi:[1,0,0]\n"
    "v_pk_fma_f32 v[188:189], v[188:189], v[178:179], v[178:179] op_sel_hi:[1,0,0]\n"
    "v_pk_fma_f32 v[190:191], v[190:191], v[178:179], v[178:179] op_sel_hi:[1,0,0]\n"
    "v_rcp_f32 v184, v184\n"
    "v_rcp_f32 v185, v185\n"
    "v_rcp_f32 v186, v186\n"
    "v_rcp_f32 v187, v187\n"
    "v_rcp_f32 v188, v188\n"
    "v_rcp_f32 v189, v189\n"
    "v_rcp_f32 v190, v190\n"
    "v_rcp_f32 v191, v191\n"
    "s_add_u32 s72, s72, 114688\n"
    "s_addc_u32 s73, s73, 0\n"
    "v_pk_mul_f32 v[48:49], v[48:49], v[184:185]\n"
    "v_pk_mul_f32 v[50:51], v[50:51], v[186:187]\n"
    "v_pk_mul_f32 v[52:53], v[52:53], v[188:189]\n"
    "v_pk_mul_f32 v[54:55], v[54:55], v[190:191]\n"
    "v_cvt_pk_bf16_f32 v192, v48, v49\n"
    "v_cvt_pk_bf16_f32 v193, v50, v51\n"
    "v_cvt_pk_bf16_f32 v194, v52, v53\n"
    "v_cvt_pk_bf16_f32 v195, v54, v55\n"
    "global_store_dwordx4 v220, v[192:195], s[72:73] offset:0 sc1\n"
    "v_pk_mul_f32 v[184:185], v[112:113], v[176:177] op_sel_hi:[1,0]\n"
    "v_pk_mul_f32 v[186:187], v[114:115], v[176:177] op_sel_hi:[1,0]\n"
    "v_pk_mul_f32 v[188:189], v[116:117], v[176:177] op_sel_hi:[1,0]\n"
    "v_pk_mul_f32 v[190:191], v[118:119], v[176:177] op_sel_hi:[1,0]\n"
    "v_mfma_f32_32x32x16_bf16 v[128:143], v[148:151], v[164:167], v[128:143]\n"
    "v_exp_f32 v184, v184\n"
    "v_exp_f32 v185, v185\n"
    "v_exp_f32 v186, v186\n"
    "v_exp_f32 v187, v187\n"
    "v_exp_f32 v188, v188\n"
    "v_exp_f32 v189, v189\n"
    "v_exp_f32 v190, v190\n"
    "v_exp_f32 v191, v191\n"
    "v_pk_mul_f32 v[112:113], v[112:113], v[120:121]\n"
    "v_pk_mul_f32 v[114:115], v[114:115], v[122:123]\n"
    "v_pk_mul_f32 v[116:117], v[116:117], v[124:125]\n"
    "v_pk_mul_f32 v[118:119], v[118:119], v[126:127]\n"
    "v_pk_fma_f32 v[184:185], v[184:185], v[178:179], v[178:179] op_sel_hi:[1,0,0]\n"
    "v_pk_fma_f32 v[186:187], v[186:187], v[178:179], v[178:179] op_sel_hi:[1,0,0]\n"
    "v_pk_fma_f32 v[188:189], v[188:189], v[178:179], v[178:179] op_sel_hi:[1,0,0]\n"
    "v_pk_fma_f32 v[190:191], v[190:191], v[178:179], v[178:179] op_sel_hi:[1,0,0]\n"
    "v_rcp_f32 v184, v184\n"
    "v_rcp_f32 v185, v185\n"
    "v_rcp_f32 v186, v186\n"
    "v_rcp_f32 v187, v187\n"
    "v_rcp_f32 v188, v188\n"
    "v_rcp_f32 v189, v189\n"
    "v_rcp_f32 v190, v190\n"
    "v_rcp_f32 v191, v191\n"
    "v_pk_mul_f32 v[112:113], v[112:113], v[184:185]\n"
    "v_pk_mul_f32 v[114:115], v[114:115], v[186:187]\n"
    "v_pk_mul_f32 v[116:117], v[116:117], v[188:189]\n"
    "v_pk_mul_f32 v[118:119], v[118:119], v[190:191]\n"
    "v_cvt_pk_bf16_f32 v196, v112, v113\n"
    "v_cvt_pk_bf16_f32 v197, v114, v115\n"
    "v_cvt_pk_bf16_f32 v198, v116, v117\n"
    "v_cvt_pk_bf16_f32 v199, v118, v119\n"
    "global_store_dwordx4 v220, v[196:199], s[72:73] offset:1024 sc1\n"
    "v_pk_mul_f32 v[184:185], v[64:65], v[180:181] op_sel_hi:[1,0]\n"
    "v_pk_mul_f32 v[186:187], v[66:67], v[180:181] op_sel_hi:[1,0]\n"
    "v_pk_mul_f32 v[188:189], v[68:69], v[180:181] op_sel_hi:[1,0]\n"
    "v_pk_mul_f32 v[190:191], v[70:71], v[180:181] op_sel_hi:[1,0]\n"
    "v_exp_f32 v184, v184\n"
    "v_exp_f32 v185, v185\n"
    "v_exp_f32 v186, v186\n"
    "v_exp_f32 v187, v187\n"
    "v_exp_f32 v188, v188\n"
    "v_exp_f32 v189, v189\n"
    "v_exp_f32 v190, v190\n"
    "v_exp_f32 v191, v191\n"
    "v_pk_mul_f32 v[64:65], v[64:65], v[72:73]\n"
    "v_pk_mul_f32 v[66:67], v[66:67], v[74:75]\n"
    "v_pk_mul_f32 v[68:69], v[68:69], v[76:77]\n"
    "v_pk_mul_f32 v[70:71], v[70:71], v[78:79]\n"
    "v_pk_fma_f32 v[184:185], v[184:185], v[182:183], v[182:183] op_sel_hi:[1,0,0]\n"
    "v_pk_fma_f32 v[186:187], v[186:187], v[182:183], v[182:183] op_sel_hi:[1,0,0]\n"
    "v_pk_fma_f32 v[188:189], v[188:189], v[182:183], v[182:183] op_sel_hi:[1,0,0]\n"
    "v_pk_fma_f32 v[190:191], v[190:191], v[182:183], v[182:183] op_sel_hi:[1,0,0]\n"
    "v_rcp_f32 v184, v184\n"
    "v_rcp_f32 v185, v185\n"
    "v_rcp_f32 v186, v186\n"
    "v_rcp_f32 v187, v187\n"
    "v_rcp_f32 v188, v188\n"
    "v_rcp_f32 v189, v189\n"
    "v_rcp_f32 v190, v190\n"
    "v_rcp_f32 v191, v191\n"
    "s_add_u32 s72, s72, 114688\n"
    "s_addc_u32 s73, s73, 0\n"
    "v_pk_mul_f32 v[64:65], v[64:65], v[184:185]\n"
    "v_pk_mul_f32 v[66:67], v[66:67], v[186:187]\n"
    "v_pk_mul_f32 v[68:69], v[68:69], v[188:189]\n"
    "v_pk_mul_f32 v[70:71], v[70:71], v[190:191]\n"
    "v_cvt_pk_bf16_f32 v192, v64, v65\n"
    "v_cvt_pk_bf16_f32 v193, v66, v67\n"
    "v_cvt_pk_bf16_f32 v194, v68, v69\n"
    "v_cvt_pk_bf16_f32 v195, v70, v71\n"
    "global_store_dwordx4 v220, v[192:195], s[72:73] offset:0 sc1\n"
    "v_pk_mul_f32 v[184:185], v[128:129], v[180:181] op_sel_hi:[1,0]\n"
    "v_pk_mul_f32 v[186:187], v[130:131], v[180:181] op_sel_hi:[1,0]\n"
    "v_pk_mul_f32 v[188:189], v[132:133], v[180:181] op_sel_hi:[1,0]\n"
    "v_pk_mul_f32 v[190:191], v[134:135], v[180:181] op_sel_hi:[1,0]\n"
    "v_exp_f32 v184, v184\n"
    "v_exp_f32 v185, v185\n"
    "v_exp_f32 v186, v186\n"
    "v_exp_f32 v187, v187\n"
    "v_exp_f32 v188, v188\n"
    "v_exp_f32 v189, v189\n"
    "v_exp_f32 v190, v190\n"
    "v_exp_f32 v191, v191\n"
    "v_pk_mul_f32 v[128:129], v[128:129], v[136:137]\n"
    "v_pk_mul_f32 v[130:131], v[130:131], v[138:139]\n"
    "v_pk_mul_f32 v[132:133], v[132:133], v[140:141]\n"
    "v_pk_mul_f32 v[134:135], v[134:135], v[142:143]\n"
    "v_pk_fma_f32 v[184:185], v[184:185], v[182:183], v[182:183] op_sel_hi:[1,0,0]\n"
    "v_pk_fma_f32 v[186:187], v[186:187], v[182:183], v[182:183] op_sel_hi:[1,0,0]\n"
    "v_pk_fma_f32 v[188:189], v[188:189], v[182:183], v[182:183] op_sel_hi:[1,0,0]\n"
    "v_pk_fma_f32 v[190:191], v[190:191], v[182:183], v[182:183] op_sel_hi:[1,0,0]\n"
    "v_rcp_f32 v184, v184\n"
    "v_rcp_f32 v185, v185\n"
    "v_rcp_f32 v186, v186\n"
    "v_rcp_f32 v187, v187\n"
    "v_rcp_f32 v188, v188\n"
    "v_rcp_f32 v189, v189\n"
    "v_rcp_f32 v190, v190\n"
    "v_rcp_f32 v191, v191\n"
    "v_pk_mul_f32 v[128:129], v[128:129], v[184:185]\n"
    "v_pk_mul_f32 v[130:131], v[130:131], v[186:187]\n"
    "v_pk_mul_f32 v[132:133], v[132:133], v[188:189]\n"
    "v_pk_mul_f32 v[134:135], v[134:135], v[190:191]\n"
    "v_cvt_pk_bf16_f32 v196, v128, v129\n"
    "v_cvt_pk_bf16_f32 v197, v130, v131\n"
    "v_cvt_pk_bf16_f32 v198, v132, v133\n"
    "v_cvt_pk_bf16_f32 v199, v134, v135\n"
    "global_store_dwordx4 v220, v[196:199], s[72:73] offset:1024 sc1\n"
    "v_mov_b32 v184, s78\n"
    "v_mov_b32 v185, s79\n"
    "v_mov_b32 v186, s77\n"
    "s_mov_b64 exec, 1\n"
    "s_waitcnt vmcnt(0)\n"
    "global_store_dwordx2 v186, v[184:185], s[80:81] sc1\n"
    "s_mov_b64 exec, -1\n"
            : "+{v[208:223]}"(vi), "+{s[64:79]}"(si), "+{s[88:91]}"(pf) : "{s[80:81]}"(ws), "{s[82:83]}"(w1), "{s[84:85]}"(w3), "{s86}"(y & 7) : "memory", "scc", "v16", "v17", "v18", "v19", "v20", "v21", "v22", "v23", "v24", "v25", "v26", "v27", "v28", "v29", "v30", "v31", "v32", "v33", "v34", "v35", "v36", "v37", "v38", "v39", "v40", "v41", "v42", "v43", "v44", "v45", "v46", "v47", "v48", "v49", "v50", "v51", "v52", "v53", "v54", "v55", "v56", "v57", "v58", "v59", "v60", "v61", "v62", "v63", "v64", "v65", "v66", "v67", "v68", "v69", "v70", "v71", "v72", "v73", "v74", "v75", "v76", "v77", "v78", "v79", "v80", "v81", "v82", "v83", "v84", "v85", "v86", "v87", "v88", "v89", "v90", "v91", "v92", "v93", "v94", "v95", "v96", "v97", "v98", "v99", "v100", "v101", "v102", "v103", "v104", "v105", "v106", "v107", "v108", "v109", "v110", "v111", "v112", "v113", "v114", "v115", "v116", "v117", "v118", "v119", "v120", "v121", "v122", "v123", "v124", "v125", "v126", "v127", "v128", "v129", "v130", "v131", "v132", "v133", "v134", "v135", "v136", "v137", "v138", "v139", "v140", "v141", "v142", "v143", "v144", "v145", "v146", "v147", "v148", "v149", "v150", "v151", "v152", "v153", "v154", "v155", "v156", "v157", "v158", "v159", "v160", "v161", "v162", "v163", "v164", "v165", "v166", "v167", "v168", "v169", "v170", "v171", "v172", "v173", "v174", "v175", "v176", "v177", "v178", "v179", "v180", "v181", "v182", "v183", "v184", "v185", "v186", "v187", "v188", "v189", "v190", "v191", "v192", "v193", "v194", "v195", "v196", "v197", "v198", "v199", "v200", "v201", "v202", "v203", "v204", "v205", "v206", "v207", "v224", "v225", "v226", "v227", "v228", "v229", "v230", "v231", "v232", "v233", "v234", "v235", "v236", "v237", "v238", "v239", "v240", "v241", "v242", "v243", "s45", "s46", "s47", "s48", "s62", "s63", "vcc");
        else asm volatile(
    "v_mov_b32 v200, v217\n"
    "v_mov_b32 v201, v217\n"
    "v_mov_b32 v202, v217\n"
    "v_mov_b32 v203, v217\n"
    "s_mov_b32 s45, 1\n"
    "s_mov_b32 s46, s86\n"
    "s_lshl_b32 s47, s46, 14\n"
    "v_add_u32 v224, s47, v208\n"
    "v_add_u32 v225, s47, v209\n"
    "v_add_u32 v226, s47, v210\n"
    "v_add_u32 v227, s47, v211\n"
    "s_add_u32 s46, s46, 1\n"
    "s_and_b32 s46, s46, 7\n"
    "s_mul_i32 s48, s46, 229376\n"
    "s_lshl_b32 s47, s46, 14\n"
    "s_add_u32 s64, s82, s48\n"
    "s_addc_u32 s65, s83, 0\n"
    "s_add_u32 s66, s84, s48\n"
    "s_addc_u32 s67, s85, 0\n"
    "s_waitcnt vmcnt(6)\n"
    "ds_read_b64_tr_b16 v[176:177], v212 offset:0\n"
    "ds_read_b64_tr_b16 v[178:179], v213 offset:0\n"
    "ds_read_b128 v[144:147], v224 offset:0\n"
    "ds_read_b128 v[148:151], v224 offset:4096\n"
    "ds_read_b128 v[152:155], v224 offset:8192\n"
    "ds_read_b128 v[156:159], v224 offset:12288\n"
    "v_add_u32 v224, s47, v208\n"
    "s_setprio 0\n"
    "s_waitcnt vmcnt(4) lgkmcnt(0)\n"
    "v_mfma_f32_32x32x16_bf16 v[16:31], v[176:179], v[144:147], 0\n"
    "v_mfma_f32_16x16x32_bf16 v[192:195], v[200:203], v[176:179], 0\n"
    "ds_read_b64_tr_b16 v[184:185], v212 offset:1024\n"
    "ds_read_b64_tr_b16 v[186:187], v213 offset:1024\n"
    "ds_read_b128 v[160:163], v225 offset:0\n"
    "v_mfma_f32_32x32x16_bf16 v[32:47], v[176:179], v[148:151], 0\n"
    "ds_read_b128 v[164:167], v225 offset:4096\n"
    "ds_read_b128 v[168:171], v225 offset:8192\n"
    "ds_read_b128 v[172:175], v225 offset:12288\n"
    "s_add_u32 m0, s68, 0\n"
    "s_mov_b64 exec, s[70:71]\n"
    "global_load_lds_dwordx4 v215, s[64:65]\n"
    "s_mov_b64 exec, -1\n"
    "v_mfma_f32_32x32x16_bf16 v[48:63], v[176:179], v[152:155], 0\n"
    "s_add_u32 m0, s68, 512\n"
    "s_mov_b64 exec, s[70:71]\n"
    "global_load_lds_dwordx4 v216, s[66:67]\n"
    "s_mov_b64 exec, -1\n"
    "s_add_u32 s64, s64, 57344\n"
    "s_addc_u32 s65, s65, 0\n"
    "s_add_u32 s66, s66, 57344\n"
    "s_addc_u32 s67, s67, 0\n"
    "v_mfma_f32_32x32x16_bf16 v[64:79], v[176:179], v[156:159], 0\n"
    "s_waitcnt vmcnt(4) lgkmcnt(0)\n"
    "v_mfma_f32_32x32x16_bf16 v[16:31], v[184:187], v[160:163], v[16:31]\n"
    "v_mfma_f32_16x16x32_bf16 v[192:195], v[200:203], v[184:187], v[192:195]\n"
    "ds_read_b64_tr_b16 v[176:177], v212 offset:2048\n"
    "ds_read_b64_tr_b16 v[178:179], v213 offset:2048\n"
    "ds_read_b128 v[144:147], v226 offset:0\n"
    "v_mfma_f32_32x32x16_bf16 v[32:47], v[184:187], v[164:167], v[32:47]\n"
    "ds_read_b128 v[148:151], v226 offset:4096\n"
    "ds_read_b128 v[152:155], v226 offset:8192\n"
    "ds_read_b128 v[156:159], v226 offset:12288\n"
    "s_add_u32 m0, s68, 1024\n"
    "s_mov_b64 exec, s[70:71]\n"
    "global_load_lds_dwordx4 v215, s[64:65]\n"
    "s_mov_b64 exec, -1\n"
    "v_mfma_f32_32x32x16_bf16 v[48:63], v[184:187], v[168:171], v[48:63]\n"
    "s_add_u32 m0, s68, 1536\n"
    "s_mov_b64 exec, s[70:71]\n"
    "global_load_lds_dwordx4 v216, s[66:67]\n"
    "s_mov_b64 exec, -1\n"
    "s_add_u32 s64, s64, 57344\n"
    "s_addc_u32 s65, s65, 0\n"
    "s_add_u32 s66, s66, 57344\n"
    "s_addc_u32 s67, s67, 0\n"
    "v_mfma_f32_32x32x16_bf16 v[64:79], v[184:187], v[172:175], v[64:79]\n"
    "s_waitcnt vmcnt(4) lgkmcnt(0)\n"
    "v_mfma_f32_32x32x16_bf16 v[16:31], v[176:179], v[144:147], v[16:31]\n"
    "v_mfma_f32_16x16x32_bf16 v[192:195], v[200:203], v[176:179], v[192:195]\n"
    "ds_read_b64_tr_b16 v[184:185], v212 offset:3072\n"
    "ds_read_b64_tr_b16 v[186:187], v213 offset:3072\n"
    "ds_read_b128 v[160:163], v227 offset:0\n"
    "v_mfma_f32_32x32x16_bf16 v[32:47], v[176:179], v[148:151], v[32:47]\n"
    "ds_read_b128 v[164:167], v227 offset:4096\n"
    "ds_read_b128 v[168:171], v227 offset:8192\n"
    "ds_read_b128 v[172:175], v227 offset:12288\n"
    "s_add_u32 m0, s68, 2048\n"
    "s_mov_b64 exec, s[70:71]\n"
    "global_load_lds_dwordx4 v215, s[64:65]\n"
    "s_mov_b64 exec, -1\n"
    "v_mfma_f32_32x32x16_bf16 v[48:63], v[176:179], v[152:155], v[48:63]\n"
    "s_add_u32 m0, s68, 2560\n"
    "s_mov_b64 exec, s[70:71]\n"
    "global_load_lds_dwordx4 v216, s[66:67]\n"
    "s_mov_b64 exec, -1\n"
    "s_add_u32 s64, s64, 57344\n"
    "s_addc_u32 s65, s65, 0\n"
    "s_add_u32 s66, s66, 57344\n"
    "s_addc_u32 s67, s67, 0\n"
    "v_mfma_f32_32x32x16_bf16 v[64:79], v[176:179], v[156:159], v[64:79]\n"
    "s_waitcnt vmcnt(4) lgkmcnt(0)\n"
    "v_mfma_f32_32x32x16_bf16 v[16:31], v[184:187], v[160:163], v[16:31]\n"
    "v_mfma_f32_16x16x32_bf16 v[192:195], v[200:203], v[184:187], v[192:195]\n"
    "ds_read_b64_tr_b16 v[176:177], v212 offset:0\n"
    "ds_read_b64_tr_b16 v[178:179], v213 offset:0\n"
    "ds_read_b128 v[144:147], v224 offset:0\n"
    "v_mfma_f32_32x32x16_bf16 v[32:47], v[184:187], v[164:167], v[32:47]\n"
    "ds_read_b128 v[148:151], v224 offset:4096\n"
    "ds_read_b128 v[152:155], v224 offset:8192\n"
    "ds_read_b128 v[156:159], v224 offset:12288\n"
    "s_add_u32 m0, s68, 3072\n"
    "s_mov_b64 exec, s[70:71]\n"
    "global_load_lds_dwordx4 v215, s[64:65]\n"
    "s_mov_b64 exec, -1\n"
    "v_mfma_f32_32x32x16_bf16 v[48:63], v[184:187], v[168:171], v[48:63]\n"
    "s_add_u32 m0, s68, 3584\n"
    "s_mov_b64 exec, s[70:71]\n"
    "global_load_lds_dwordx4 v216, s[66:67]\n"
    "s_mov_b64 exec, -1\n"
    "s_setprio 3\n"
    "v_add_u32 v225, s47, v209\n"
    "v_add_u32 v226, s47, v210\n"
    "v_add_u32 v227, s47, v211\n"
    "s_add_u32 s46, s46, 1\n"
    "s_and_b32 s46, s46, 7\n"
    "s_mul_i32 s48, s46, 229376\n"
    "s_lshl_b32 s47, s46, 14\n"
    "s_add_u32 s64, s82, s48\n"
    "s_addc_u32 s65, s83, 0\n"
    "s_add_u32 s66, s84, s48\n"
    "s_addc_u32 s67, s85, 0\n"
    "v_add_u32 v224, s47, v208\n"
    "v_permlane16_swap_b32 v192, v193\n"
    "v_mov_b32 v194, s78\n"
    "v_mov_b32 v195, s79\n"
    "v_mov_b32 v193, v192\n"
    "s_mov_b64 exec, 0xffffffff\n"
    "global_store_dwordx4 v214, v[192:195], s[80:81] sc1\n"
    "s_mov_b64 exec, -1\n"
    "s_setprio 0\n"
    "v_mfma_f32_32x32x16_bf16 v[64:79], v[184:187], v[172:175], v[64:79]\n"
    "1:\n"
    "s_waitcnt vmcnt(4) lgkmcnt(0)\n"
    "v_mfma_f32_32x32x16_bf16 v[16:31], v[176:179], v[144:147], v[16:31]\n"
    "ds_read_b64_tr_b16 v[184:185], v212 offset:1024\n"
    "ds_read_b64_tr_b16 v[186:187], v213 offset:1024\n"
    "ds_read_b128 v[160:163], v225 offset:0\n"
    "v_mfma_f32_32x32x16_bf16 v[32:47], v[176:179], v[148:151], v[32:47]\n"
    "ds_read_b128 v[164:167], v225 offset:4096\n"
    "ds_read_b128 v[168:171], v225 offset:8192\n"
    "ds_read_b128 v[172:175], v225 offset:12288\n"
    "s_add_u32 m0, s68, 0\n"
    "s_mov_b64 exec, s[70:71]\n"
    "global_load_lds_dwordx4 v215, s[64:65]\n"
    "s_mov_b64 exec, -1\n"
    "v_mfma_f32_32x32x16_bf16 v[48:63], v[176:179], v[152:155], v[48:63]\n"
    "s_add_u32 m0, s68, 512\n"
    "s_mov_b64 exec, s[70:71]\n"
    "global_load_lds_dwordx4 v216, s[66:67]\n"
    "s_mov_b64 exec, -1\n"
    "s_add_u32 s64, s64, 57344\n"
    "s_addc_u32 s65, s65, 0\n"
    "s_add_u32 s66, s66, 57344\n"
    "s_addc_u32 s67, s67, 0\n"
    "s_cmp_eq_u32 s45, 7\n"
    "s_cbranch_scc0 2f\n"
    "global_load_dwordx4 v[228:231], v219, s[80:81] offset:0 sc1\n"
    "global_load_dwordx4 v[232:235], v219, s[80:81] offset:512 sc1\n"
    "global_load_dwordx4 v[236:239], v219, s[80:81] offset:1024 sc1\n"
    "global_load_dwordx4 v[240:243], v219, s[80:81] offset:1536 sc1\n"
    "2:\n"
    "v_mfma_f32_32x32x16_bf16 v[64:79], v[176:179], v[156:159], v[64:79]\n"
    "s_waitcnt vmcnt(4) lgkmcnt(0)\n"
    "v_mfma_f32_32x32x16_bf16 v[16:31], v[184:187], v[160:163], v[16:31]\n"
    "ds_read_b64_tr_b16 v[176:177], v212 offset:2048\n"
    "ds_read_b64_tr_b16 v[178:179], v213 offset:2048\n"
    "ds_read_b128 v[144:147], v226 offset:0\n"
    "v_mfma_f32_32x32x16_bf16 v[32:47], v[184:187], v[164:167], v[32:47]\n"
    "ds_read_b128 v[148:151], v226 offset:4096\n"
    "ds_read_b128 v[152:155], v226 offset:8192\n"
    "ds_read_b128 v[156:159], v226 offset:12288\n"
    "s_add_u32 m0, s68, 1024\n"
    "s_mov_b64 exec, s[70:71]\n"
    "global_load_lds_dwordx4 v215, s[64:65]\n"
    "s_mov_b64 exec, -1\n"
    "v_mfma_f32_32x32x16_bf16 v[48:63], v[184:187], v[168:171], v[48:63]\n"
    "s_add_u32 m0, s68, 1536\n"
    "s_mov_b64 exec, s[70:71]\n"
    "global_load_lds_dwordx4 v216, s[66:67]\n"
    "s_mov_b64 exec, -1\n"
    "s_add_u32 s64, s64, 57344\n"
    "s_addc_u32 s65, s65, 0\n"
    "s_add_u32 s66, s66, 57344\n"
    "s_addc_u32 s67, s67, 0\n"
    "v_mfma_f32_32x32x16_bf16 v[64:79], v[184:187], v[172:175], v[64:79]\n"
    "s_waitcnt vmcnt(4) lgkmcnt(0)\n"
    "v_mfma_f32_32x32x16_bf16 v[16:31], v[176:179], v[144:147], v[16:31]\n"
    "ds_read_b64_tr_b16 v[184:185], v212 offset:3072\n"
    "ds_read_b64_tr_b16 v[186:187], v213 offset:3072\n"
    "ds_read_b128 v[160:163], v227 offset:0\n"
    "v_mfma_f32_32x32x16_bf16 v[32:47], v[176:179], v[148:151], v[32:47]\n"
    "ds_read_b128 v[164:167], v227 offset:4096\n"
    "ds_read_b128 v[168:171], v227 offset:8192\n"
    "ds_read_b128 v[172:175], v227 offset:12288\n"
    "s_add_u32 m0, s68, 2048\n"
    "s_mov_b64 exec, s[70:71]\n"
    "global_load_lds_dwordx4 v215, s[64:65]\n"
    "s_mov_b64 exec, -1\n"
    "v_mfma_f32_32x32x16_bf16 v[48:63], v[176:179], v[152:155], v[48:63]\n"
    "s_add_u32 m0, s68, 2560\n"
    "s_mov_b64 exec, s[70:71]\n"
    "global_load_lds_dwordx4 v216, s[66:67]\n"
    "s_mov_b64 exec, -1\n"
    "s_add_u32 s64, s64, 57344\n"
    "s_addc_u32 s65, s65, 0\n"
    "s_add_u32 s66, s66, 57344\n"
    "s_addc_u32 s67, s67, 0\n"
    "v_mfma_f32_32x32x16_bf16 v[64:79], v[176:179], v[156:159], v[64:79]\n"
    "s_waitcnt vmcnt(4) lgkmcnt(0)\n"
    "v_mfma_f32_32x32x16_bf16 v[16:31], v[184:187], v[160:163], v[16:31]\n"
    "ds_read_b64_tr_b16 v[176:177], v212 offset:0\n"
    "ds_read_b64_tr_b16 v[178:179], v213 offset:0\n"
    "ds_read_b128 v[144:147], v224 offset:0\n"
    "v_mfma_f32_32x32x16_bf16 v[32:47], v[184:187], v[164:167], v[32:47]\n"
    "ds_read_b128 v[148:151], v224 offset:4096\n"
    "ds_read_b128 v[152:155], v224 offset:8192\n"
    "ds_read_b128 v[156:159], v224 offset:12288\n"
    "s_add_u32 m0, s68, 3072\n"
    "s_mov_b64 exec, s[70:71]\n"
    "global_load_lds_dwordx4 v215, s[64:65]\n"
    "s_mov_b64 exec, -1\n"
    "v_mfma_f32_32x32x16_bf16 v[48:63], v[184:187], v[168:171], v[48:63]\n"
    "s_add_u32 m0, s68, 3584\n"
    "s_mov_b64 exec, s[70:71]\n"
    "global_load_lds_dwordx4 v216, s[66:67]\n"
    "s_mov_b64 exec, -1\n"
    "s_setprio 3\n"
    "v_add_u32 v225, s47, v209\n"
    "v_add_u32 v226, s47, v210\n"
    "v_add_u32 v227, s47, v211\n"
    "s_add_u32 s46, s46, 1\n"
    "s_and_b32 s46, s46, 7\n"
    "s_mul_i32 s48, s46, 229376\n"
    "s_lshl_b32 s47, s46, 14\n"
    "s_add_u32 s64, s82, s48\n"
    "s_addc_u32 s65, s83, 0\n"
    "s_add_u32 s66, s84, s48\n"
    "s_addc_u32 s67, s85, 0\n"
    "v_add_u32 v224, s47, v208\n"
    "s_setprio 0\n"
    "s_add_u32 s45, s45, 1\n"
    "s_cmp_lt_u32 s45, 8\n"
    "v_mfma_f32_32x32x16_bf16 v[64:79], v[184:187], v[172:175], v[64:79]\n"
    "s_cbranch_scc1 1b\n"
    "s_setprio 3\n"
    "s_waitcnt vmcnt(0) lgkmcnt(0)\n"
    "s_mov_b64 exec, 1\n"
    "ds_add_u32 v222, v223\n"
    "s_mov_b64 exec, -1\n"
    "ds_read_b32 v168, v218 offset:0\n"
    "ds_read_b32 v152, v218 offset:512\n"
    "ds_read_b32 v172, v218 offset:128\n"
    "ds_read_b32 v156, v218 offset:640\n"
    "ds_read_b32 v176, v218 offset:256\n"
    "ds_read_b32 v160, v218 offset:768\n"
    "ds_read_b32 v180, v218 offset:384\n"
    "ds_read_b32 v164, v218 offset:896\n"
    "s_mov_b32 s45, 0x40000\n"
    "3:\n"
    "v_cmp_ne_u64 vcc, s[78:79], v[230:231]\n"
    "v_cmp_ne_u64 s[46:47], s[78:79], v[234:235]\n"
    "s_nop 0\n"
    "s_or_b64 vcc, vcc, s[46:47]\n"
    "v_cmp_ne_u64 s[46:47], s[78:79], v[238:239]\n"
    "s_nop 0\n"
    "s_or_b64 vcc, vcc, s[46:47]\n"
    "v_cmp_ne_u64 s[46:47], s[78:79], v[242:243]\n"
    "s_nop 0\n"
    "s_or_b64 vcc, vcc, s[46:47]\n"
    "s_cbranch_scc0 4f\n"
    "s_sub_u32 s45, s45, 1\n"
    "s_cbranch_scc1 4f\n"
    "global_load_dwordx4 v[228:231], v219, s[80:81] offset:0 sc1\n"
    "global_load_dwordx4 v[232:235], v219, s[80:81] offset:512 sc1\n"
    "global_load_dwordx4 v[236:239], v219, s[80:81] offset:1024 sc1\n"
    "global_load_dwordx4 v[240:243], v219, s[80:81] offset:1536 sc1\n"
    "s_waitcnt vmcnt(0)\n"
    "s_branch 3b\n"
    "4:\n"
    "v_pk_add_f32 v[228:229], v[228:229], v[232:233]\n"
    "v_pk_add_f32 v[236:237], v[236:237], v[240:241]\n"
    "v_mov_b64 v[144:145], 0\n"
    "v_mov_b64 v[146:147], 0\n"
    "v_mov_b64 v[148:149], 0\n"
    "v_mov_b64 v[150:151], 0\n"
    "v_pk_add_f32 v[228:229], v[228:229], v[236:237]\n"
    "v_mov_b32 v153, 0\n"
    "v_mov_b64 v[154:155], 0\n"
    "v_mov_b32 v157, 0\n"
    "v_mov_b64 v[158:159], 0\n"
    "v_mov_b32 v161, 0\n"
    "v_mov_b64 v[162:163], 0\n"
    "v_mov_b32 v165, 0\n"
    "v_mov_b64 v[166:167], 0\n"
    "v_mov_b64 v[230:231], v[228:229]\n"
    "s_nop 1\n"
    "v_permlane32_swap_b32 v228, v230\n"
    "v_permlane32_swap_b32 v229, v231\n"
    "v_pk_add_f32 v[228:229], v[228:229], v[230:231]\n"
    "s_mov_b64 exec, 0xffffffff\n"
    "v_cvt_pk_bf16_f32 v184, v228, v228\n"
    "v_lshlrev_b32 v184, 16, v184\n"
    "v_sub_f32 v184, v228, v184\n"
    "v_cvt_pk_bf16_f32 v144, v228, v184\n"
    "s_mov_b64 exec, -1\n"
    "s_waitcnt lgkmcnt(0)\n"
    "v_mul_f32 v170, v168, v168\n"
    "v_mul_f32 v174, v172, v172\n"
    "v_mul_f32 v178, v176, v176\n"
    "v_mul_f32 v182, v180, v180\n"
    "v_mfma_f32_32x32x16_bf16 v[16:31], v[144:147], v[152:155], v[16:31]\n"
    "v_rcp_f32 v170, v170\n"
    "v_rcp_f32 v174, v174\n"
    "v_rcp_f32 v178, v178\n"
    "v_rcp_f32 v182, v182\n"
    "v_mfma_f32_32x32x16_bf16 v[32:47], v[144:147], v[156:159], v[32:47]\n"
    "v_mul_f32 v168, 0xbfb8aa3b, v168\n"
    "v_mul_f32 v172, 0xbfb8aa3b, v172\n"
    "v_mul_f32 v176, 0xbfb8aa3b, v176\n"
    "v_mul_f32 v180, 0xbfb8aa3b, v180\n"
    "s_nop 2\n"
    "v_add_u32 v204, 0x2000, v221\n"
    "v_pk_mul_f32 v[184:185], v[16:17], v[168:169] op_sel_hi:[1,0]\n"
    "v_pk_mul_f32 v[186:187], v[18:19], v[168:169] op_sel_hi:[1,0]\n"
    "v_pk_mul_f32 v[188:189], v[20:21], v[168:169] op_sel_hi:[1,0]\n"
    "v_pk_mul_f32 v[190:191], v[22:23], v[168:169] op_sel_hi:[1,0]\n"
    "v_mfma_f32_32x32x16_bf16 v[48:63], v[144:147], v[160:163], v[48:63]\n"
    "v_exp_f32 v184, v184\n"
    "v_exp_f32 v185, v185\n"
    "v_exp_f32 v186, v186\n"
    "v_exp_f32 v187, v187\n"
    "v_exp_f32 v188, v188\n"
    "v_exp_f32 v189, v189\n"
    "v_exp_f32 v190, v190\n"
    "v_exp_f32 v191, v191\n"
    "v_pk_mul_f32 v[16:17], v[16:17], v[24:25]\n"
    "v_pk_mul_f32 v[18:19], v[18:19], v[26:27]\n"
    "v_pk_mul_f32 v[20:21], v[20:21], v[28:29]\n"
    "v_pk_mul_f32 v[22:23], v[22:23], v[30:31]\n"
    "v_pk_fma_f32 v[184:185], v[184:185], v[170:171], v[170:171] op_sel_hi:[1,0,0]\n"
    "v_pk_fma_f32 v[186:187], v[186:187], v[170:171], v[170:171] op_sel_hi:[1,0,0]\n"
    "v_pk_fma_f32 v[188:189], v[188:189], v[170:171], v[170:171] op_sel_hi:[1,0,0]\n"
    "v_pk_fma_f32 v[190:191], v[190:191], v[170:171], v[170:171] op_sel_hi:[1,0,0]\n"
    "v_rcp_f32 v184, v184\n"
    "v_rcp_f32 v185, v185\n"
    "v_rcp_f32 v186, v186\n"
    "v_rcp_f32 v187, v187\n"
    "v_rcp_f32 v188, v188\n"
    "v_rcp_f32 v189, v189\n"
    "v_rcp_f32 v190, v190\n"
    "v_rcp_f32 v191, v191\n"
    "v_pk_mul_f32 v[16:17], v[16:17], v[184:185]\n"
    "v_pk_mul_f32 v[18:19], v[18:19], v[186:187]\n"
    "v_pk_mul_f32 v[20:21], v[20:21], v[188:189]\n"
    "v_pk_mul_f32 v[22:23], v[22:23], v[190:191]\n"
    "v_cvt_pk_bf16_f32 v192, v16, v17\n"
    "v_cvt_pk_bf16_f32 v193, v18, v19\n"
    "v_cvt_pk_bf16_f32 v194, v20, v21\n"
    "v_cvt_pk_bf16_f32 v195, v22, v23\n"
    "global_store_dwordx4 v220, v[192:195], s[72:73] offset:0 sc1\n"
    "v_pk_mul_f32 v[184:185], v[32:33], v[172:173] op_sel_hi:[1,0]\n"
    "v_pk_mul_f32 v[186:187], v[34:35], v[172:173] op_sel_hi:[1,0]\n"
    "v_pk_mul_f32 v[188:189], v[36:37], v[172:173] op_sel_hi:[1,0]\n"
    "v_pk_mul_f32 v[190:191], v[38:39], v[172:173] op_sel_hi:[1,0]\n"
    "v_mfma_f32_32x32x16_bf16 v[64:79], v[144:147], v[164:167], v[64:79]\n"
    "v_exp_f32 v184, v184\n"
    "v_exp_f32 v185, v185\n"
    "v_exp_f32 v186, v186\n"
    "v_exp_f32 v187, v187\n"
    "v_exp_f32 v188, v188\n"
    "v_exp_f32 v189, v189\n"
    "v_exp_f32 v190, v190\n"
    "v_exp_f32 v191, v191\n"
    "v_pk_mul_f32 v[32:33], v[32:33], v[40:41]\n"
    "v_pk_mul_f32 v[34:35], v[34:35], v[42:43]\n"
    "v_pk_mul_f32 v[36:37], v[36:37], v[44:45]\n"
    "v_pk_mul_f32 v[38:39], v[38:39], v[46:47]\n"
    "v_pk_fma_f32 v[184:185], v[184:185], v[174:175], v[174:175] op_sel_hi:[1,0,0]\n"
    "v_pk_fma_f32 v[186:187], v[186:187], v[174:175], v[174:175] op_sel_hi:[1,0,0]\n"
    "v_pk_fma_f32 v[188:189], v[188:189], v[174:175], v[174:175] op_sel_hi:[1,0,0]\n"
    "v_pk_fma_f32 v[190:191], v[190:191], v[174:175], v[174:175] op_sel_hi:[1,0,0]\n"
    "v_rcp_f32 v184, v184\n"
    "v_rcp_f32 v185, v185\n"
    "v_rcp_f32 v186, v186\n"
    "v_rcp_f32 v187, v187\n"
    "v_rcp_f32 v188, v188\n"
    "v_rcp_f32 v189, v189\n"
    "v_rcp_f32 v190, v190\n"
    "v_rcp_f32 v191, v191\n"
    "s_add_u32 s72, s72, 114688\n"
    "s_addc_u32 s73, s73, 0\n"
    "v_pk_mul_f32 v[32:33], v[32:33], v[184:185]\n"
    "v_pk_mul_f32 v[34:35], v[34:35], v[186:187]\n"
    "v_pk_mul_f32 v[36:37], v[36:37], v[188:189]\n"
    "v_pk_mul_f32 v[38:39], v[38:39], v[190:191]\n"
    "v_cvt_pk_bf16_f32 v196, v32, v33\n"
    "v_cvt_pk_bf16_f32 v197, v34, v35\n"
    "v_cvt_pk_bf16_f32 v198, v36, v37\n"
    "v_cvt_pk_bf16_f32 v199, v38, v39\n"
    "global_store_dwordx4 v220, v[196:199], s[72:73] offset:0 sc1\n"
    "v_pk_mul_f32 v[184:185], v[48:49], v[176:177] op_sel_hi:[1,0]\n"
    "v_pk_mul_f32 v[186:187], v[50:51], v[176:177] op_sel_hi:[1,0]\n"
    "v_pk_mul_f32 v[188:189], v[52:53], v[176:177] op_sel_hi:[1,0]\n"
    "v_pk_mul_f32 v[190:191], v[54:55], v[176:177] op_sel_hi:[1,0]\n"
    "v_exp_f32 v184, v184\n"
    "v_exp_f32 v185, v185\n"
    "v_exp_f32 v186, v186\n"
    "v_exp_f32 v187, v187\n"
    "v_exp_f32 v188, v188\n"
    "v_exp_f32 v189, v189\n"
    "v_exp_f32 v190, v190\n"
    "v_exp_f32 v191, v191\n"
    "v_pk_mul_f32 v[48:49], v[48:49], v[56:57]\n"
    "v_pk_mul_f32 v[50:51], v[50:51], v[58:59]\n"
    "v_pk_mul_f32 v[52:53], v[52:53], v[60:61]\n"
    "v_pk_mul_f32 v[54:55], v[54:55], v[62:63]\n"
    "v_pk_fma_f32 v[184:185], v[184:185], v[178:179], v[178:179] op_sel_hi:[1,0,0]\n"
    "v_pk_fma_f32 v[186:187], v[186:187], v[178:179], v[178:179] op_sel_hi:[1,0,0]\n"
    "v_pk_fma_f32 v[188:189], v[188:189], v[178:179], v[178:179] op_sel_hi:[1,0,0]\n"
    "v_pk_fma_f32 v[190:191], v[190:191], v[178:179], v[178:179] op_sel_hi:[1,0,0]\n"
    "v_rcp_f32 v184, v184\n"
    "v_rcp_f32 v185, v185\n"
    "v_rcp_f32 v186, v186\n"
    "v_rcp_f32 v187, v187\n"
    "v_rcp_f32 v188, v188\n"
    "v_rcp_f32 v189, v189\n"
    "v_rcp_f32 v190, v190\n"
    "v_rcp_f32 v191, v191\n"
    "s_add_u32 s72, s72, 114688\n"
    "s_addc_u32 s73, s73, 0\n"
    "v_pk_mul_f32 v[48:49], v[48:49], v[184:185]\n"
    "v_pk_mul_f32 v[50:51], v[50:51], v[186:187]\n"
    "v_pk_mul_f32 v[52:53], v[52:53], v[188:189]\n"
    "v_pk_mul_f32 v[54:55], v[54:55], v[190:191]\n"
    "v_cvt_pk_bf16_f32 v192, v48, v49\n"
    "v_cvt_pk_bf16_f32 v193, v50, v51\n"
    "v_cvt_pk_bf16_f32 v194, v52, v53\n"
    "v_cvt_pk_bf16_f32 v195, v54, v55\n"
    "global_store_dwordx4 v220, v[192:195], s[72:73] offset:0 sc1\n"
    "v_pk_mul_f32 v[184:185], v[64:65], v[180:181] op_sel_hi:[1,0]\n"
    "v_pk_mul_f32 v[186:187], v[66:67], v[180:181] op_sel_hi:[1,0]\n"
    "v_pk_mul_f32 v[188:189], v[68:69], v[180:181] op_sel_hi:[1,0]\n"
    "v_pk_mul_f32 v[190:191], v[70:71], v[180:181] op_sel_hi:[1,0]\n"
    "v_exp_f32 v184, v184\n"
    "v_exp_f32 v185, v185\n"
    "v_exp_f32 v186, v186\n"
    "v_exp_f32 v187, v187\n"
    "v_exp_f32 v188, v188\n"
    "v_exp_f32 v189, v189\n"
    "v_exp_f32 v190, v190\n"
    "v_exp_f32 v191, v191\n"
    "v_pk_mul_f32 v[64:65], v[64:65], v[72:73]\n"
    "v_pk_mul_f32 v[66:67], v[66:67], v[74:75]\n"
    "v_pk_mul_f32 v[68:69], v[68:69], v[76:77]\n"
    "v_pk_mul_f32 v[70:71], v[70:71], v[78:79]\n"
    "v_pk_fma_f32 v[184:185], v[184:185], v[182:183], v[182:183] op_sel_hi:[1,0,0]\n"
    "v_pk_fma_f32 v[186:187], v[186:187], v[182:183], v[182:183] op_sel_hi:[1,0,0]\n"
    "v_pk_fma_f32 v[188:189], v[188:189], v[182:183], v[182:183] op_sel_hi:[1,0,0]\n"
    "v_pk_fma_f32 v[190:191], v[190:191], v[182:183], v[182:183] op_sel_hi:[1,0,0]\n"
    "v_rcp_f32 v184, v184\n"
    "v_rcp_f32 v185, v185\n"
    "v_rcp_f32 v186, v186\n"
    "v_rcp_f32 v187, v187\n"
    "v_rcp_f32 v188, v188\n"
    "v_rcp_f32 v189, v189\n"
    "v_rcp_f32 v190, v190\n"
    "v_rcp_f32 v191, v191\n"
    "s_add_u32 s72, s72, 114688\n"
    "s_addc_u32 s73, s73, 0\n"
    "v_pk_mul_f32 v[64:65], v[64:65], v[184:185]\n"
    "v_pk_mul_f32 v[66:67], v[66:67], v[186:187]\n"
    "v_pk_mul_f32 v[68:69], v[68:69], v[188:189]\n"
    "v_pk_mul_f32 v[70:71], v[70:71], v[190:191]\n"
    "v_cvt_pk_bf16_f32 v196, v64, v65\n"
    "v_cvt_pk_bf16_f32 v197, v66, v67\n"
    "v_cvt_pk_bf16_f32 v198, v68, v69\n"
    "v_cvt_pk_bf16_f32 v199, v70, v71\n"
    "global_store_dwordx4 v220, v[196:199], s[72:73] offset:0 sc1\n"
    "v_mov_b32 v184, s78\n"
    "v_mov_b32 v185, s79\n"
    "v_mov_b32 v186, s77\n"
    "s_mov_b64 exec, 1\n"
    "s_waitcnt vmcnt(0)\n"
    "global_store_dwordx2 v186, v[184:185], s[80:81] sc1\n"
    "s_mov_b64 exec, -1\n"
    "v_lshlrev_b32 v188, 3, v220\n"
    "global_load_dword v189, v188, s[88:89]\n"
    "s_add_u32 s88, s88, 8192\n"
    "s_addc_u32 s89, s89, 0\n"
    "s_sleep 6\n"
    "global_load_dword v189, v188, s[88:89]\n"
    "s_add_u32 s88, s88, 8192\n"
    "s_addc_u32 s89, s89, 0\n"
    "s_sleep 6\n"
    "global_load_dword v189, v188, s[88:89]\n"
    "s_add_u32 s88, s88, 8192\n"
    "s_addc_u32 s89, s89, 0\n"
    "s_sleep 6\n"
    "global_load_dword v189, v188, s[88:89]\n"
    "s_add_u32 s88, s88, 8192\n"
    "s_addc_u32 s89, s89, 0\n"
    "s_sleep 6\n"
    "global_load_dword v189, v188, s[88:89]\n"
    "s_add_u32 s88, s88, 8192\n"
    "s_addc_u32 s89, s89, 0\n"
    "s_sleep 6\n"
    "global_load_dword v189, v188, s[88:89]\n"
    "s_add_u32 s88, s88, 8192\n"
    "s_addc_u32 s89, s89, 0\n"
    "s_sleep 6\n"
    "global_load_dword v189, v188, s[88:89]\n"
    "s_add_u32 s88, s88, 8192\n"
    "s_addc_u32 s89, s89, 0\n"
    "s_sleep 6\n"
    "global_load_dword v189, v188, s[90:91]\n"
    "s_add_u32 s90, s90, 8192\n"
    "s_addc_u32 s91, s91, 0\n"
    "s_sleep 6\n"
    "global_load_dword v189, v188, s[90:91]\n"
    "s_add_u32 s90, s90, 8192\n"
    "s_addc_u32 s91, s91, 0\n"
    "s_sleep 6\n"
    "s_mov_b32 s45, 0x400000\n"
    "6:\n"
    "ds_read_b32 v205, v222\n"
    "s_sub_u32 s45, s45, 1\n"
    "s_cbranch_scc1 7f\n"
    "s_waitcnt lgkmcnt(0)\n"
    "v_readfirstlane_b32 s48, v205\n"
    "s_sub_u32 s48, s48, 0x01010101\n"
    "s_and_b32 s48, s48, 0x80808080\n"
    "s_cbranch_scc0 7f\n"
    "s_sleep 2\n"
    "s_branch 6b\n"
    "7:\n"
    "s_mov_b32 s45, 0\n"
    "8:\n"
    "s_add_u32 s46, s74, 0\n"
    "s_addc_u32 s47, s75, 0\n"
    "s_add_u32 m0, s76, 0\n"
    "s_nop 0\n"
    "global_load_lds_dwordx4 v221, s[46:47]\n"
    "s_add_u32 m0, s76, 1024\n"
    "s_nop 0\n"
    "global_load_lds_dwordx4 v204, s[46:47]\n"
    "s_add_u32 s46, s74, 458752\n"
    "s_addc_u32 s47, s75, 0\n"
    "s_add_u32 m0, s76, 32768\n"
    "s_nop 0\n"
    "global_load_lds_dwordx4 v221, s[46:47]\n"
    "s_add_u32 m0, s76, 33792\n"
    "s_nop 0\n"
    "global_load_lds_dwordx4 v204, s[46:47]\n"
    "s_add_u32 s46, s74, 917504\n"
    "s_addc_u32 s47, s75, 0\n"
    "s_add_u32 m0, s76, 65536\n"
    "s_nop 0\n"
    "global_load_lds_dwordx4 v221, s[46:47]\n"
    "s_add_u32 m0, s76, 66560\n"
    "s_nop 0\n"
    "global_load_lds_dwordx4 v204, s[46:47]\n"
    "s_add_u32 s46, s74, 1376256\n"
    "s_addc_u32 s47, s75, 0\n"
    "s_add_u32 m0, s76, 98304\n"
    "s_nop 0\n"
    "global_load_lds_dwordx4 v221, s[46:47]\n"
    "s_add_u32 m0, s76, 99328\n"
    "s_nop 0\n"
    "global_load_lds_dwordx4 v204, s[46:47]\n"
    "s_add_u32 s74, s74, 16384\n"
    "s_addc_u32 s75, s75, 0\n"
    "s_add_u32 s76, s76, 2048\n"
    "s_add_u32 s45, s45, 1\n"
    "s_cmp_lt_u32 s45, 16\n"
    "s_waitcnt vmcnt(32)\n"
    "s_cbranch_scc1 8b\n"
    "v_mov_b32 v184, 1\n"
    "s_mov_b64 exec, 1\n"
    "s_waitcnt vmcnt(0)\n"
    "ds_write_b32 v222, v184 offset:4\n"
    "s_mov_b64 exec, -1\n"
            : "+{v[208:223]}"(vi), "+{s[64:79]}"(si), "+{s[88:91]}"(pf) : "{s[80:81]}"(ws), "{s[82:83]}"(w1), "{s[84:85]}"(w3), "{s86}"(y & 7) : "memory", "scc", "v16", "v17", "v18", "v19", "v20", "v21", "v22", "v23", "v24", "v25", "v26", "v27", "v28", "v29", "v30", "v31", "v32", "v33", "v34", "v35", "v36", "v37", "v38", "v39", "v40", "v41", "v42", "v43", "v44", "v45", "v46", "v47", "v48", "v49", "v50", "v51", "v52", "v53", "v54", "v55", "v56", "v57", "v58", "v59", "v60", "v61", "v62", "v63", "v64", "v65", "v66", "v67", "v68", "v69", "v70", "v71", "v72", "v73", "v74", "v75", "v76", "v77", "v78", "v79", "v80", "v81", "v82", "v83", "v84", "v85", "v86", "v87", "v88", "v89", "v90", "v91", "v92", "v93", "v94", "v95", "v96", "v97", "v98", "v99", "v100", "v101", "v102", "v103", "v104", "v105", "v106", "v107", "v108", "v109", "v110", "v111", "v112", "v113", "v114", "v115", "v116", "v117", "v118", "v119", "v120", "v121", "v122", "v123", "v124", "v125", "v126", "v127", "v128", "v129", "v130", "v131", "v132", "v133", "v134", "v135", "v136", "v137", "v138", "v139", "v140", "v141", "v142", "v143", "v144", "v145", "v146", "v147", "v148", "v149", "v150", "v151", "v152", "v153", "v154", "v155", "v156", "v157", "v158", "v159", "v160", "v161", "v162", "v163", "v164", "v165", "v166", "v167", "v168", "v169", "v170", "v171", "v172", "v173", "v174", "v175", "v176", "v177", "v178", "v179", "v180", "v181", "v182", "v183", "v184", "v185", "v186", "v187", "v188", "v189", "v190", "v191", "v192", "v193", "v194", "v195", "v196", "v197", "v198", "v199", "v200", "v201", "v202", "v203", "v204", "v205", "v206", "v207", "v224", "v225", "v226", "v227", "v228", "v229", "v230", "v231", "v232", "v233", "v234", "v235", "v236", "v237", "v238", "v239", "v240", "v241", "v242", "v243", "s45", "s46", "s47", "s48", "s62", "s63", "vcc");
    }
    STAMP(4); LST(30);
    {
        int ln = lane; OPAQUE(ln);
        const int r2 = ln & 31, h2 = ln >> 5, q2 = (ln & 15) >> 2;
        const int rbk = 4 * xcd + (y >> 3), cb = y & 7, tt = g >> 1, kh = g & 1;
        const unsigned ring = L_X + g * 32768, v2a = (ln >> 3) * 1024 + cb * 128 + (((ln & 7) ^ (((ln >> 4) & 1) << 2)) << 4);
        const unsigned wr0 = (4 * h2 + q2) * 128 + ((q2 >> 1) & 1) * 64 + ((ln >> 4) & 1) * 32 + (ln & 3) * 8, orow = (unsigned)(64 * rbk + 32 * tt + r2) * 1024u + cb * 128 + kh * 64;
        u32x16 vi = {v2a, v2a + 8192, orow + h2 * 16, WS_FH + (((rbk >> 1) * 16 + 4 * g) * 4 + (ln & 15)) * 128, (unsigned)ln * 16, ring + wr0, ring + (wr0 ^ 64), L_P, 1u << (8 * g), orow + h2 * 8,
                     ring + 8192 + ln * 16, L_X + 8192 + (2 * kh + tt) * 4096 + ln * 16, WS_FO + (rbk * 32 + 16 * kh + (ln & 15)) * 128, L_X + kh * 65536 + (wr0 ^ (kh * 64)), L_X + (g ^ 1) * 32768 + 8192 + ln * 16, 0u};
        const char* w2b = w2 + (unsigned)g * (448u * 1024u) + 16 * 16384; const char* wob = wo + (unsigned)g * 131072u;
        const char* h0 = ws + WS_H + (unsigned)(2 * rbk * 112 + 28 * g + 4) * 1024u; const char* h1 = h0 + 112 * 1024; const char* ob = ws + WS_O + (unsigned)((2 * rbk + tt) * 32 + 16 * kh + 4) * 1024u;
        u32x16 si = {LO(w2b), HI(w2b), LO(wob), HI(wob), ring, g == 0 ? L_DBG + 160 + 124 : L_DUM, LO(ws), HI(ws), (unsigned)ep, (unsigned)(ep >> 32), LO(h0), HI(h0), LO(h1), HI(h1), LO(x), HI(x)};
        u32x8 sj = {LO(out), HI(out), LO(ob), HI(ob), 4u << (8 * (g ^ 1)), WS_FH + ((bi * 16 + bj) * 4 + g) * 128, 0u, 0u};
#pragma unroll
        for (int e = 0; e < 16; ++e) si[e] = __builtin_amdgcn_readfirstlane(si[e]);
#pragma unroll
        for (int e = 0; e < 8; ++e) sj[e] = __builtin_amdgcn_readfirstlane(sj[e]);
        asm volatile(
    "s_mov_b32 s55, 0x40000\n"
    "10:\n"
    "global_load_dwordx2 v[204:205], v211, s[70:71] sc1\n"
    "s_sub_u32 s55, s55, 1\n"
    "s_cbranch_scc1 11f\n"
    "s_waitcnt vmcnt(0)\n"
    "v_cmp_ne_u64 vcc, s[72:73], v[204:205]\n"
    "s_cbranch_vccz 11f\n"
    "s_sleep 1\n"
    "s_branch 10b\n"
    "11:\n"
    "global_load_dwordx4 v[96:99], v212, s[74:75] offset:-4096 sc1\n"
    "global_load_dwordx4 v[100:103], v212, s[76:77] offset:-4096 sc1\n"
    "global_load_dwordx4 v[104:107], v212, s[74:75] offset:-3072 sc1\n"
    "global_load_dwordx4 v[108:111], v212, s[76:77] offset:-3072 sc1\n"
    "global_load_dwordx4 v[112:115], v212, s[74:75] offset:-2048 sc1\n"
    "global_load_dwordx4 v[116:119], v212, s[76:77] offset:-2048 sc1\n"
    "global_load_dwordx4 v[120:123], v212, s[74:75] offset:-1024 sc1\n"
    "global_load_dwordx4 v[124:127], v212, s[76:77] offset:-1024 sc1\n"
    "s_mov_b32 s55, 0x400000\n"
    "8:\n"
    "ds_read_b32 v206, v215 offset:4\n"
    "s_sub_u32 s55, s55, 1\n"
    "s_cbranch_scc1 9f\n"
    "s_waitcnt lgkmcnt(0)\n"
    "v_readfirstlane_b32 s51, v206\n"
    "s_cmp_eq_u32 s51, 0\n"
    "s_cbranch_scc0 9f\n"
    "s_sleep 1\n"
    "s_branch 8b\n"
    "9:\n"
    "ds_read_b64_tr_b16 v[80:81], v213 offset:0\n"
    "ds_read_b64_tr_b16 v[82:83], v213 offset:1024\n"
    "ds_read_b64_tr_b16 v[84:85], v214 offset:0\n"
    "ds_read_b64_tr_b16 v[86:87], v214 offset:1024\n"
    "s_setprio 0\n"
    "s_waitcnt vmcnt(6) lgkmcnt(0)\n"
    "v_mfma_f32_32x32x16_bf16 v[16:31], v[80:83], v[96:99], 0\n"
    "ds_read_b64_tr_b16 v[88:89], v213 offset:2048\n"
    "ds_read_b64_tr_b16 v[90:91], v213 offset:3072\n"
    "ds_read_b64_tr_b16 v[92:93], v214 offset:2048\n"
    "ds_read_b64_tr_b16 v[94:95], v214 offset:3072\n"
    "v_mfma_f32_32x32x16_bf16 v[32:47], v[80:83], v[100:103], 0\n"
    "s_add_u32 m0, s68, 0\n"
    "s_nop 0\n"
    "global_load_lds_dwordx4 v208, s[64:65]\n"
    "v_mfma_f32_32x32x16_bf16 v[48:63], v[84:87], v[96:99], 0\n"
    "s_add_u32 m0, s68, 1024\n"
    "s_nop 0\n"
    "global_load_lds_dwordx4 v209, s[64:65]\n"
    "s_add_u32 s64, s64, 16384\n"
    "s_addc_u32 s65, s65, 0\n"
    "v_mfma_f32_32x32x16_bf16 v[64:79], v[84:87], v[100:103], 0\n"
    "global_load_dwordx4 v[128:131], v212, s[74:75] offset:0 sc1\n"
    "global_load_dwordx4 v[132:135], v212, s[76:77] offset:0 sc1\n"
    "global_load_dwordx4 v[136:139], v212, s[74:75] offset:1024 sc1\n"
    "global_load_dwordx4 v[140:143], v212, s[76:77] offset:1024 sc1\n"
    "s_waitcnt vmcnt(10) lgkmcnt(0)\n"
    "v_mfma_f32_32x32x16_bf16 v[16:31], v[88:91], v[104:107], v[16:31]\n"
    "ds_read_b64_tr_b16 v[80:81], v213 offset:4096\n"
    "ds_read_b64_tr_b16 v[82:83], v213 offset:5120\n"
    "ds_read_b64_tr_b16 v[84:85], v214 offset:4096\n"
    "ds_read_b64_tr_b16 v[86:87], v214 offset:5120\n"
    "v_mfma_f32_32x32x16_bf16 v[32:47], v[88:91], v[108:111], v[32:47]\n"
    "s_add_u32 m0, s68, 2048\n"
    "s_nop 0\n"
    "global_load_lds_dwordx4 v208, s[64:65]\n"
    "v_mfma_f32_32x32x16_bf16 v[48:63], v[92:95], v[104:107], v[48:63]\n"
    "s_add_u32 m0, s68, 3072\n"
    "s_nop 0\n"
    "global_load_lds_dwordx4 v209, s[64:65]\n"
    "s_add_u32 s64, s64, 16384\n"
    "s_addc_u32 s65, s65, 0\n"
    "v_mfma_f32_32x32x16_bf16 v[64:79], v[92:95], v[108:111], v[64:79]\n"
    "global_load_dwordx4 v[144:147], v212, s[74:75] offset:2048 sc1\n"
    "global_load_dwordx4 v[148:151], v212, s[76:77] offset:2048 sc1\n"
    "global_load_dwordx4 v[152:155], v212, s[74:75] offset:3072 sc1\n"
    "global_load_dwordx4 v[156:159], v212, s[76:77] offset:3072 sc1\n"
    "s_waitcnt vmcnt(14) lgkmcnt(0)\n"
    "v_mfma_f32_32x32x16_bf16 v[16:31], v[80:83], v[112:115], v[16:31]\n"
    "ds_read_b64_tr_b16 v[88:89], v213 offset:6144\n"
    "ds_read_b64_tr_b16 v[90:91], v213 offset:7168\n"
    "ds_read_b64_tr_b16 v[92:93], v214 offset:6144\n"
    "ds_read_b64_tr_b16 v[94:95], v214 offset:7168\n"
    "v_mfma_f32_32x32x16_bf16 v[32:47], v[80:83], v[116:119], v[32:47]\n"
    "s_add_u32 m0, s68, 4096\n"
    "s_nop 0\n"
    "global_load_lds_dwordx4 v208, s[64:65]\n"
    "v_mfma_f32_32x32x16_bf16 v[48:63], v[84:87], v[112:115], v[48:63]\n"
    "s_add_u32 m0, s68, 5120\n"
    "s_nop 0\n"
    "global_load_lds_dwordx4 v209, s[64:65]\n"
    "s_add_u32 s64, s64, 16384\n"
    "s_addc_u32 s65, s65, 0\n"
    "v_mfma_f32_32x32x16_bf16 v[64:79], v[84:87], v[116:119], v[64:79]\n"
    "s_add_u32 s74, s74, 8192\n"
    "s_addc_u32 s75, s75, 0\n"
    "s_add_u32 s76, s76, 8192\n"
    "s_addc_u32 s77, s77, 0\n"
    "global_load_dwordx4 v[160:163], v212, s[74:75] offset:-4096 sc1\n"
    "global_load_dwordx4 v[164:167], v212, s[76:77] offset:-4096 sc1\n"
    "global_load_dwordx4 v[168:171], v212, s[74:75] offset:-3072 sc1\n"
    "global_load_dwordx4 v[172:175], v212, s[76:77] offset:-3072 sc1\n"
    "s_waitcnt vmcnt(18) lgkmcnt(0)\n"
    "v_mfma_f32_32x32x16_bf16 v[16:31], v[88:91], v[120:123], v[16:31]\n"
    "ds_read_b64_tr_b16 v[80:81], v213 offset:8192\n"
    "ds_read_b64_tr_b16 v[82:83], v213 offset:9216\n"
    "ds_read_b64_tr_b16 v[84:85], v214 offset:8192\n"
    "ds_read_b64_tr_b16 v[86:87], v214 offset:9216\n"
    "v_mfma_f32_32x32x16_bf16 v[32:47], v[88:91], v[124:127], v[32:47]\n"
    "s_add_u32 m0, s68, 6144\n"
    "s_nop 0\n"
    "global_load_lds_dwordx4 v208, s[64:65]\n"
    "v_mfma_f32_32x32x16_bf16 v[48:63], v[92:95], v[120:123], v[48:63]\n"
    "s_add_u32 m0, s68, 7168\n"
    "s_nop 0\n"
    "global_load_lds_dwordx4 v209, s[64:65]\n"
    "s_add_u32 s64, s64, 16384\n"
    "s_addc_u32 s65, s65, 0\n"
    "v_mfma_f32_32x32x16_bf16 v[64:79], v[92:95], v[124:127], v[64:79]\n"
    "global_load_dwordx4 v[176:179], v212, s[74:75] offset:-2048 sc1\n"
    "global_load_dwordx4 v[180:183], v212, s[76:77] offset:-2048 sc1\n"
    "global_load_dwordx4 v[184:187], v212, s[74:75] offset:-1024 sc1\n"
    "global_load_dwordx4 v[188:191], v212, s[76:77] offset:-1024 sc1\n"
    "s_waitcnt vmcnt(20) lgkmcnt(0)\n"
    "v_mfma_f32_32x32x16_bf16 v[16:31], v[80:83], v[128:131], v[16:31]\n"
    "ds_read_b64_tr_b16 v[88:89], v213 offset:10240\n"
    "ds_read_b64_tr_b16 v[90:91], v213 offset:11264\n"
    "ds_read_b64_tr_b16 v[92:93], v214 offset:10240\n"
    "ds_read_b64_tr_b16 v[94:95], v214 offset:11264\n"
    "v_mfma_f32_32x32x16_bf16 v[32:47], v[80:83], v[132:135], v[32:47]\n"
    "s_add_u32 m0, s68, 8192\n"
    "s_nop 0\n"
    "global_load_lds_dwordx4 v208, s[64:65]\n"
    "v_mfma_f32_32x32x16_bf16 v[48:63], v[84:87], v[128:131], v[48:63]\n"
    "s_add_u32 m0, s68, 9216\n"
    "s_nop 0\n"
    "global_load_lds_dwordx4 v209, s[64:65]\n"
    "s_add_u32 s64, s64, 16384\n"
    "s_addc_u32 s65, s65, 0\n"
    "v_mfma_f32_32x32x16_bf16 v[64:79], v[84:87], v[132:135], v[64:79]\n"
    "global_load_dwordx4 v[96:99], v212, s[74:75] offset:0 sc1\n"
    "global_load_dwordx4 v[100:103], v212, s[76:77] offset:0 sc1\n"
    "global_load_dwordx4 v[104:107], v212, s[74:75] offset:1024 sc1\n"
    "global_load_dwordx4 v[108:111], v212, s[76:77] offset:1024 sc1\n"
    "s_waitcnt vmcnt(24) lgkmcnt(0)\n"
    "v_mfma_f32_32x32x16_bf16 v[16:31], v[88:91], v[136:139], v[16:31]\n"
    "ds_read_b64_tr_b16 v[80:81], v213 offset:12288\n"
    "ds_read_b64_tr_b16 v[82:83], v213 offset:13312\n"
    "ds_read_b64_tr_b16 v[84:85], v214 offset:12288\n"
    "ds_read_b64_tr_b16 v[86:87], v214 offset:13312\n"
    "v_mfma_f32_32x32x16_bf16 v[32:47], v[88:91], v[140:143], v[32:47]\n"
    "s_add_u32 m0, s68, 10240\n"
    "s_nop 0\n"
    "global_load_lds_dwordx4 v208, s[64:65]\n"
    "v_mfma_f32_32x32x16_bf16 v[48:63], v[92:95], v[136:139], v[48:63]\n"
    "s_add_u32 m0, s68, 11264\n"
    "s_nop 0\n"
    "global_load_lds_dwordx4 v209, s[64:65]\n"
    "s_add_u32 s64, s64, 16384\n"
    "s_addc_u32 s65, s65, 0\n"
    "v_mfma_f32_32x32x16_bf16 v[64:79], v[92:95], v[140:143], v[64:79]\n"
    "global_load_dwordx4 v[112:115], v212, s[74:75] offset:2048 sc1\n"
    "global_load_dwordx4 v[116:119], v212, s[76:77] offset:2048 sc1\n"
    "global_load_dwordx4 v[120:123], v212, s[74:75] offset:3072 sc1\n"
    "global_load_dwordx4 v[124:127], v212, s[76:77] offset:3072 sc1\n"
    "s_waitcnt vmcnt(26) lgkmcnt(0)\n"
    "v_mfma_f32_32x32x16_bf16 v[16:31], v[80:83], v[144:147], v[16:31]\n"
    "ds_read_b64_tr_b16 v[88:89], v213 offset:14336\n"
    "ds_read_b64_tr_b16 v[90:91], v213 offset:15360\n"
    "ds_read_b64_tr_b16 v[92:93], v214 offset:14336\n"
    "ds_read_b64_tr_b16 v[94:95], v214 offset:15360\n"
    "v_mfma_f32_32x32x16_bf16 v[32:47], v[80:83], v[148:151], v[32:47]\n"
    "s_add_u32 m0, s68, 12288\n"
    "s_nop 0\n"
    "global_load_lds_dwordx4 v208, s[64:65]\n"
    "v_mfma_f32_32x32x16_bf16 v[48:63], v[84:87], v[144:147], v[48:63]\n"
    "s_add_u32 m0, s68, 13312\n"
    "s_nop 0\n"
    "global_load_lds_dwordx4 v209, s[64:65]\n"
    "s_add_u32 s64, s64, 16384\n"
    "s_addc_u32 s65, s65, 0\n"
    "v_mfma_f32_32x32x16_bf16 v[64:79], v[84:87], v[148:151], v[64:79]\n"
    "s_add_u32 s74, s74, 8192\n"
    "s_addc_u32 s75, s75, 0\n"
    "s_add_u32 s76, s76, 8192\n"
    "s_addc_u32 s77, s77, 0\n"
    "global_load_dwordx4 v[128:131], v212, s[74:75] offset:-4096 sc1\n"
    "global_load_dwordx4 v[132:135], v212, s[76:77] offset:-4096 sc1\n"
    "global_load_dwordx4 v[136:139], v212, s[74:75] offset:-3072 sc1\n"
    "global_load_dwordx4 v[140:143], v212, s[76:77] offset:-3072 sc1\n"
    "s_waitcnt vmcnt(30) lgkmcnt(0)\n"
    "v_mfma_f32_32x32x16_bf16 v[16:31], v[88:91], v[152:155], v[16:31]\n"
    "ds_read_b64_tr_b16 v[80:81], v213 offset:16384\n"
    "ds_read_b64_tr_b16 v[82:83], v213 offset:17408\n"
    "ds_read_b64_tr_b16 v[84:85], v214 offset:16384\n"
    "ds_read_b64_tr_b16 v[86:87], v214 offset:17408\n"
    "v_mfma_f32_32x32x16_bf16 v[32:47], v[88:91], v[156:159], v[32:47]\n"
    "s_add_u32 m0, s68, 14336\n"
    "s_nop 0\n"
    "global_load_lds_dwordx4 v208, s[64:65]\n"
    "v_mfma_f32_32x32x16_bf16 v[48:63], v[92:95], v[152:155], v[48:63]\n"
    "s_add_u32 m0, s68, 15360\n"
    "s_nop 0\n"
    "global_load_lds_dwordx4 v209, s[64:65]\n"
    "s_add_u32 s64, s64, 16384\n"
    "s_addc_u32 s65, s65, 0\n"
    "v_mfma_f32_32x32x16_bf16 v[64:79], v[92:95], v[156:159], v[64:79]\n"
    "global_load_dwordx4 v[144:147], v212, s[74:75] offset:-2048 sc1\n"
    "global_load_dwordx4 v[148:151], v212, s[76:77] offset:-2048 sc1\n"
    "global_load_dwordx4 v[152:155], v212, s[74:75] offset:-1024 sc1\n"
    "global_load_dwordx4 v[156:159], v212, s[76:77] offset:-1024 sc1\n"
    "s_waitcnt vmcnt(32) lgkmcnt(0)\n"
    "v_mfma_f32_32x32x16_bf16 v[16:31], v[80:83], v[160:163], v[16:31]\n"
    "ds_read_b64_tr_b16 v[88:89], v213 offset:18432\n"
    "ds_read_b64_tr_b16 v[90:91], v213 offset:19456\n"
    "ds_read_b64_tr_b16 v[92:93], v214 offset:18432\n"
    "ds_read_b64_tr_b16 v[94:95], v214 offset:19456\n"
    "v_mfma_f32_32x32x16_bf16 v[32:47], v[80:83], v[164:167], v[32:47]\n"
    "s_add_u32 m0, s68, 16384\n"
    "s_nop 0\n"
    "global_load_lds_dwordx4 v208, s[64:65]\n"
    "v_mfma_f32_32x32x16_bf16 v[48:63], v[84:87], v[160:163], v[48:63]\n"
    "s_add_u32 m0, s68, 17408\n"
    "s_nop 0\n"
    "global_load_lds_dwordx4 v209, s[64:65]\n"
    "s_add_u32 s64, s64, 16384\n"
    "s_addc_u32 s65, s65, 0\n"
    "v_mfma_f32_32x32x16_bf16 v[64:79], v[84:87], v[164:167], v[64:79]\n"
    "global_load_dwordx4 v[160:163], v212, s[74:75] offset:0 sc1\n"
    "global_load_dwordx4 v[164:167], v212, s[76:77] offset:0 sc1\n"
    "s_waitcnt vmcnt(34) lgkmcnt(0)\n"
    "v_mfma_f32_32x32x16_bf16 v[16:31], v[88:91], v[168:171], v[16:31]\n"
    "ds_read_b64_tr_b16 v[80:81], v213 offset:20480\n"
    "ds_read_b64_tr_b16 v[82:83], v213 offset:21504\n"
    "ds_read_b64_tr_b16 v[84:85], v214 offset:20480\n"
    "ds_read_b64_tr_b16 v[86:87], v214 offset:21504\n"
    "v_mfma_f32_32x32x16_bf16 v[32:47], v[88:91], v[172:175], v[32:47]\n"
    "s_add_u32 m0, s68, 18432\n"
    "s_nop 0\n"
    "global_load_lds_dwordx4 v208, s[64:65]\n"
    "v_mfma_f32_32x32x16_bf16 v[48:63], v[92:95], v[168:171], v[48:63]\n"
    "s_add_u32 m0, s68, 19456\n"
    "s_nop 0\n"
    "global_load_lds_dwordx4 v209, s[64:65]\n"
    "s_add_u32 s64, s64, 16384\n"
    "s_addc_u32 s65, s65, 0\n"
    "v_mfma_f32_32x32x16_bf16 v[64:79], v[92:95], v[172:175], v[64:79]\n"
    "global_load_dwordx4 v[168:171], v212, s[74:75] offset:1024 sc1\n"
    "global_load_dwordx4 v[172:175], v212, s[76:77] offset:1024 sc1\n"
    "s_waitcnt vmcnt(34) lgkmcnt(0)\n"
    "v_mfma_f32_32x32x16_bf16 v[16:31], v[80:83], v[176:179], v[16:31]\n"
    "ds_read_b64_tr_b16 v[88:89], v213 offset:22528\n"
    "ds_read_b64_tr_b16 v[90:91], v213 offset:23552\n"
    "ds_read_b64_tr_b16 v[92:93], v214 offset:22528\n"
    "ds_read_b64_tr_b16 v[94:95], v214 offset:23552\n"
    "v_mfma_f32_32x32x16_bf16 v[32:47], v[80:83], v[180:183], v[32:47]\n"
    "s_add_u32 m0, s68, 20480\n"
    "s_nop 0\n"
    "global_load_lds_dwordx4 v208, s[64:65]\n"
    "v_mfma_f32_32x32x16_bf16 v[48:63], v[84:87], v[176:179], v[48:63]\n"
    "s_add_u32 m0, s68, 21504\n"
    "s_nop 0\n"
    "global_load_lds_dwordx4 v209, s[64:65]\n"
    "s_add_u32 s64, s64, 16384\n"
    "s_addc_u32 s65, s65, 0\n"
    "v_mfma_f32_32x32x16_bf16 v[64:79], v[84:87], v[180:183], v[64:79]\n"
    "global_load_dwordx4 v[176:179], v212, s[74:75] offset:2048 sc1\n"
    "global_load_dwordx4 v[180:183], v212, s[76:77] offset:2048 sc1\n"
    "s_waitcnt vmcnt(36) lgkmcnt(0)\n"
    "v_mfma_f32_32x32x16_bf16 v[16:31], v[88:91], v[184:187], v[16:31]\n"
    "ds_read_b64_tr_b16 v[80:81], v213 offset:24576\n"
    "ds_read_b64_tr_b16 v[82:83], v213 offset:25600\n"
    "ds_read_b64_tr_b16 v[84:85], v214 offset:24576\n"
    "ds_read_b64_tr_b16 v[86:87], v214 offset:25600\n"
    "v_mfma_f32_32x32x16_bf16 v[32:47], v[88:91], v[188:191], v[32:47]\n"
    "s_add_u32 m0, s68, 22528\n"
    "s_nop 0\n"
    "global_load_lds_dwordx4 v208, s[64:65]\n"
    "v_mfma_f32_32x32x16_bf16 v[48:63], v[92:95], v[184:187], v[48:63]\n"
    "s_add_u32 m0, s68, 23552\n"
    "s_nop 0\n"
    "global_load_lds_dwordx4 v209, s[64:65]\n"
    "s_add_u32 s64, s64, 16384\n"
    "s_addc_u32 s65, s65, 0\n"
    "v_mfma_f32_32x32x16_bf16 v[64:79], v[92:95], v[188:191], v[64:79]\n"
    "global_load_dwordx4 v[184:187], v212, s[74:75] offset:3072 sc1\n"
    "global_load_dwordx4 v[188:191], v212, s[76:77] offset:3072 sc1\n"
    "s_waitcnt vmcnt(36) lgkmcnt(0)\n"
    "v_mfma_f32_32x32x16_bf16 v[16:31], v[80:83], v[96:99], v[16:31]\n"
    "ds_read_b64_tr_b16 v[88:89], v213 offset:26624\n"
    "ds_read_b64_tr_b16 v[90:91], v213 offset:27648\n"
    "ds_read_b64_tr_b16 v[92:93], v214 offset:26624\n"
    "ds_read_b64_tr_b16 v[94:95], v214 offset:27648\n"
    "v_mfma_f32_32x32x16_bf16 v[32:47], v[80:83], v[100:103], v[32:47]\n"
    "s_add_u32 m0, s68, 24576\n"
    "s_nop 0\n"
    "global_load_lds_dwordx4 v208, s[66:67]\n"
    "v_mfma_f32_32x32x16_bf16 v[48:63], v[84:87], v[96:99], v[48:63]\n"
    "s_add_u32 m0, s68, 25600\n"
    "s_nop 0\n"
    "global_load_lds_dwordx4 v209, s[66:67]\n"
    "s_add_u32 s66, s66, 16384\n"
    "s_addc_u32 s67, s67, 0\n"
    "v_mfma_f32_32x32x16_bf16 v[64:79], v[84:87], v[100:103], v[64:79]\n"
    "s_add_u32 s74, s74, 8192\n"
    "s_addc_u32 s75, s75, 0\n"
    "s_add_u32 s76, s76, 8192\n"
    "s_addc_u32 s77, s77, 0\n"
    "global_load_dwordx4 v[96:99], v212, s[74:75] offset:-4096 sc1\n"
    "global_load_dwordx4 v[100:103], v212, s[76:77] offset:-4096 sc1\n"
    "s_waitcnt vmcnt(38) lgkmcnt(0)\n"
    "v_mfma_f32_32x32x16_bf16 v[16:31], v[88:91], v[104:107], v[16:31]\n"
    "ds_read_b64_tr_b16 v[80:81], v213 offset:28672\n"
    "ds_read_b64_tr_b16 v[82:83], v213 offset:29696\n"
    "ds_read_b64_tr_b16 v[84:85], v214 offset:28672\n"
    "ds_read_b64_tr_b16 v[86:87], v214 offset:29696\n"
    "v_mfma_f32_32x32x16_bf16 v[32:47], v[88:91], v[108:111], v[32:47]\n"
    "s_add_u32 m0, s68, 26624\n"
    "s_nop 0\n"
    "global_load_lds_dwordx4 v208, s[66:67]\n"
    "v_mfma_f32_32x32x16_bf16 v[48:63], v[92:95], v[104:107], v[48:63]\n"
    "s_add_u32 m0, s68, 27648\n"
    "s_nop 0\n"
    "global_load_lds_dwordx4 v209, s[66:67]\n"
    "s_add_u32 s66, s66, 16384\n"
    "s_addc_u32 s67, s67, 0\n"
    "v_mfma_f32_32x32x16_bf16 v[64:79], v[92:95], v[108:111], v[64:79]\n"
    "global_load_dwordx4 v[104:107], v212, s[74:75] offset:-3072 sc1\n"
    "global_load_dwordx4 v[108:111], v212, s[76:77] offset:-3072 sc1\n"
    "s_waitcnt vmcnt(38) lgkmcnt(0)\n"
    "v_mfma_f32_32x32x16_bf16 v[16:31], v[80:83], v[112:115], v[16:31]\n"
    "ds_read_b64_tr_b16 v[88:89], v213 offset:30720\n"
    "ds_read_b64_tr_b16 v[90:91], v213 offset:31744\n"
    "ds_read_b64_tr_b16 v[92:93], v214 offset:30720\n"
    "ds_read_b64_tr_b16 v[94:95], v214 offset:31744\n"
    "v_mfma_f32_32x32x16_bf16 v[32:47], v[80:83], v[116:119], v[32:47]\n"
    "s_add_u32 m0, s68, 28672\n"
    "s_nop 0\n"
    "global_load_lds_dwordx4 v208, s[66:67]\n"
    "v_mfma_f32_32x32x16_bf16 v[48:63], v[84:87], v[112:115], v[48:63]\n"
    "s_add_u32 m0, s68, 29696\n"
    "s_nop 0\n"
    "global_load_lds_dwordx4 v209, s[66:67]\n"
    "s_add_u32 s66, s66, 16384\n"
    "s_addc_u32 s67, s67, 0\n"
    "v_mfma_f32_32x32x16_bf16 v[64:79], v[84:87], v[116:119], v[64:79]\n"
    "global_load_dwordx4 v[112:115], v212, s[74:75] offset:-2048 sc1\n"
    "global_load_dwordx4 v[116:119], v212, s[76:77] offset:-2048 sc1\n"
    "s_waitcnt vmcnt(40) lgkmcnt(0)\n"
    "v_mfma_f32_32x32x16_bf16 v[16:31], v[88:91], v[120:123], v[16:31]\n"
    "ds_read_b64_tr_b16 v[80:81], v213 offset:0\n"
    "ds_read_b64_tr_b16 v[82:83], v213 offset:1024\n"
    "ds_read_b64_tr_b16 v[84:85], v214 offset:0\n"
    "ds_read_b64_tr_b16 v[86:87], v214 offset:1024\n"
    "v_mfma_f32_32x32x16_bf16 v[32:47], v[88:91], v[124:127], v[32:47]\n"
    "s_add_u32 m0, s68, 30720\n"
    "s_nop 0\n"
    "global_load_lds_dwordx4 v208, s[66:67]\n"
    "v_mfma_f32_32x32x16_bf16 v[48:63], v[92:95], v[120:123], v[48:63]\n"
    "s_add_u32 m0, s68, 31744\n"
    "s_nop 0\n"
    "global_load_lds_dwordx4 v209, s[66:67]\n"
    "s_add_u32 s66, s66, 16384\n"
    "s_addc_u32 s67, s67, 0\n"
    "v_mfma_f32_32x32x16_bf16 v[64:79], v[92:95], v[124:127], v[64:79]\n"
    "global_load_dwordx4 v[120:123], v212, s[74:75] offset:-1024 sc1\n"
    "global_load_dwordx4 v[124:127], v212, s[76:77] offset:-1024 sc1\n"
    "s_waitcnt vmcnt(40) lgkmcnt(0)\n"
    "v_mfma_f32_32x32x16_bf16 v[16:31], v[80:83], v[128:131], v[16:31]\n"
    "ds_read_b64_tr_b16 v[88:89], v213 offset:2048\n"
    "ds_read_b64_tr_b16 v[90:91], v213 offset:3072\n"
    "ds_read_b64_tr_b16 v[92:93], v214 offset:2048\n"
    "ds_read_b64_tr_b16 v[94:95], v214 offset:3072\n"
    "v_mfma_f32_32x32x16_bf16 v[32:47], v[80:83], v[132:135], v[32:47]\n"
    "s_add_u32 m0, s68, 0\n"
    "s_nop 0\n"
    "global_load_lds_dwordx4 v208, s[66:67]\n"
    "v_mfma_f32_32x32x16_bf16 v[48:63], v[84:87], v[128:131], v[48:63]\n"
    "s_add_u32 m0, s68, 1024\n"
    "s_nop 0\n"
    "global_load_lds_dwordx4 v209, s[66:67]\n"
    "s_add_u32 s66, s66, 16384\n"
    "s_addc_u32 s67, s67, 0\n"
    "v_mfma_f32_32x32x16_bf16 v[64:79], v[84:87], v[132:135], v[64:79]\n"
    "global_load_dwordx2 v[192:193], v217, s[78:79] offset:0\n"
    "global_load_dwordx2 v[194:195], v217, s[78:79] offset:16\n"
    "global_load_dwordx2 v[196:197], v217, s[78:79] offset:32\n"
    "global_load_dwordx2 v[198:199], v217, s[78:79] offset:48\n"
    "s_waitcnt vmcnt(44) lgkmcnt(0)\n"
    "v_mfma_f32_32x32x16_bf16 v[16:31], v[88:91], v[136:139], v[16:31]\n"
    "ds_read_b64_tr_b16 v[80:81], v213 offset:4096\n"
    "ds_read_b64_tr_b16 v[82:83], v213 offset:5120\n"
    "ds_read_b64_tr_b16 v[84:85], v214 offset:4096\n"
    "ds_read_b64_tr_b16 v[86:87], v214 offset:5120\n"
    "v_mfma_f32_32x32x16_bf16 v[32:47], v[88:91], v[140:143], v[32:47]\n"
    "s_add_u32 m0, s68, 2048\n"
    "s_nop 0\n"
    "global_load_lds_dwordx4 v208, s[66:67]\n"
    "v_mfma_f32_32x32x16_bf16 v[48:63], v[92:95], v[136:139], v[48:63]\n"
    "s_add_u32 m0, s68, 3072\n"
    "s_nop 0\n"
    "global_load_lds_dwordx4 v209, s[66:67]\n"
    "s_add_u32 s66, s66, 16384\n"
    "s_addc_u32 s67, s67, 0\n"
    "v_mfma_f32_32x32x16_bf16 v[64:79], v[92:95], v[140:143], v[64:79]\n"
    "s_waitcnt vmcnt(42) lgkmcnt(0)\n"
    "v_mfma_f32_32x32x16_bf16 v[16:31], v[80:83], v[144:147], v[16:31]\n"
    "ds_read_b64_tr_b16 v[88:89], v213 offset:6144\n"
    "ds_read_b64_tr_b16 v[90:91], v213 offset:7168\n"
    "ds_read_b64_tr_b16 v[92:93], v214 offset:6144\n"
    "ds_read_b64_tr_b16 v[94:95], v214 offset:7168\n"
    "v_mfma_f32_32x32x16_bf16 v[32:47], v[80:83], v[148:151], v[32:47]\n"
    "s_add_u32 m0, s68, 4096\n"
    "s_nop 0\n"
    "global_load_lds_dwordx4 v208, s[66:67]\n"
    "v_mfma_f32_32x32x16_bf16 v[48:63], v[84:87], v[144:147], v[48:63]\n"
    "s_add_u32 m0, s68, 5120\n"
    "s_nop 0\n"
    "global_load_lds_dwordx4 v209, s[66:67]\n"
    "s_add_u32 s66, s66, 16384\n"
    "s_addc_u32 s67, s67, 0\n"
    "v_mfma_f32_32x32x16_bf16 v[64:79], v[84:87], v[148:151], v[64:79]\n"
    "s_waitcnt vmcnt(42) lgkmcnt(0)\n"
    "v_mfma_f32_32x32x16_bf16 v[16:31], v[88:91], v[152:155], v[16:31]\n"
    "ds_read_b64_tr_b16 v[80:81], v213 offset:8192\n"
    "ds_read_b64_tr_b16 v[82:83], v213 offset:9216\n"
    "ds_read_b64_tr_b16 v[84:85], v214 offset:8192\n"
    "ds_read_b64_tr_b16 v[86:87], v214 offset:9216\n"
    "v_mfma_f32_32x32x16_bf16 v[32:47], v[88:91], v[156:159], v[32:47]\n"
    "s_add_u32 m0, s68, 6144\n"
    "s_nop 0\n"
    "global_load_lds_dwordx4 v208, s[66:67]\n"
    "v_mfma_f32_32x32x16_bf16 v[48:63], v[92:95], v[152:155], v[48:63]\n"
    "s_add_u32 m0, s68, 7168\n"
    "s_nop 0\n"
    "global_load_lds_dwordx4 v209, s[66:67]\n"
    "s_add_u32 s66, s66, 16384\n"
    "s_addc_u32 s67, s67, 0\n"
    "v_mfma_f32_32x32x16_bf16 v[64:79], v[92:95], v[156:159], v[64:79]\n"
    "s_waitcnt vmcnt(40) lgkmcnt(0)\n"
    "v_mfma_f32_32x32x16_bf16 v[16:31], v[80:83], v[160:163], v[16:31]\n"
    "ds_read_b64_tr_b16 v[88:89], v213 offset:10240\n"
    "ds_read_b64_tr_b16 v[90:91], v213 offset:11264\n"
    "ds_read_b64_tr_b16 v[92:93], v214 offset:10240\n"
    "ds_read_b64_tr_b16 v[94:95], v214 offset:11264\n"
    "v_mfma_f32_32x32x16_bf16 v[32:47], v[80:83], v[164:167], v[32:47]\n"
    "v_mfma_f32_32x32x16_bf16 v[48:63], v[84:87], v[160:163], v[48:63]\n"
    "v_mfma_f32_32x32x16_bf16 v[64:79], v[84:87], v[164:167], v[64:79]\n"
    "s_waitcnt vmcnt(36) lgkmcnt(0)\n"
    "v_mfma_f32_32x32x16_bf16 v[16:31], v[88:91], v[168:171], v[16:31]\n"
    "ds_read_b64_tr_b16 v[80:81], v213 offset:12288\n"
    "ds_read_b64_tr_b16 v[82:83], v213 offset:13312\n"
    "ds_read_b64_tr_b16 v[84:85], v214 offset:12288\n"
    "ds_read_b64_tr_b16 v[86:87], v214 offset:13312\n"
    "v_mfma_f32_32x32x16_bf16 v[32:47], v[88:91], v[172:175], v[32:47]\n"
    "v_mfma_f32_32x32x16_bf16 v[48:63], v[92:95], v[168:171], v[48:63]\n"
    "v_mfma_f32_32x32x16_bf16 v[64:79], v[92:95], v[172:175], v[64:79]\n"
    "s_waitcnt vmcnt(32) lgkmcnt(0)\n"
    "v_mfma_f32_32x32x16_bf16 v[16:31], v[80:83], v[176:179], v[16:31]\n"
    "ds_read_b64_tr_b16 v[88:89], v213 offset:14336\n"
    "ds_read_b64_tr_b16 v[90:91], v213 offset:15360\n"
    "ds_read_b64_tr_b16 v[92:93], v214 offset:14336\n"
    "ds_read_b64_tr_b16 v[94:95], v214 offset:15360\n"
    "v_mfma_f32_32x32x16_bf16 v[32:47], v[80:83], v[180:183], v[32:47]\n"
    "v_mfma_f32_32x32x16_bf16 v[48:63], v[84:87], v[176:179], v[48:63]\n"
    "v_mfma_f32_32x32x16_bf16 v[64:79], v[84:87], v[180:183], v[64:79]\n"
    "s_waitcnt vmcnt(28) lgkmcnt(0)\n"
    "v_mfma_f32_32x32x16_bf16 v[16:31], v[88:91], v[184:187], v[16:31]\n"
    "ds_read_b64_tr_b16 v[80:81], v213 offset:16384\n"
    "ds_read_b64_tr_b16 v[82:83], v213 offset:17408\n"
    "ds_read_b64_tr_b16 v[84:85], v214 offset:16384\n"
    "ds_read_b64_tr_b16 v[86:87], v214 offset:17408\n"
    "v_mfma_f32_32x32x16_bf16 v[32:47], v[88:91], v[188:191], v[32:47]\n"
    "v_mfma_f32_32x32x16_bf16 v[48:63], v[92:95], v[184:187], v[48:63]\n"
    "v_mfma_f32_32x32x16_bf16 v[64:79], v[92:95], v[188:191], v[64:79]\n"
    "s_waitcnt vmcnt(24) lgkmcnt(0)\n"
    "v_mfma_f32_32x32x16_bf16 v[16:31], v[80:83], v[96:99], v[16:31]\n"
    "ds_read_b64_tr_b16 v[88:89], v213 offset:18432\n"
    "ds_read_b64_tr_b16 v[90:91], v213 offset:19456\n"
    "ds_read_b64_tr_b16 v[92:93], v214 offset:18432\n"
    "ds_read_b64_tr_b16 v[94:95], v214 offset:19456\n"
    "v_mfma_f32_32x32x16_bf16 v[32:47], v[80:83], v[100:103], v[32:47]\n"
    "v_mfma_f32_32x32x16_bf16 v[48:63], v[84:87], v[96:99], v[48:63]\n"
    "v_mfma_f32_32x32x16_bf16 v[64:79], v[84:87], v[100:103], v[64:79]\n"
    "s_waitcnt vmcnt(20) lgkmcnt(0)\n"
    "v_mfma_f32_32x32x16_bf16 v[16:31], v[88:91], v[104:107], v[16:31]\n"
    "ds_read_b64_tr_b16 v[80:81], v213 offset:20480\n"
    "ds_read_b64_tr_b16 v[82:83], v213 offset:21504\n"
    "ds_read_b64_tr_b16 v[84:85], v214 offset:20480\n"
    "ds_read_b64_tr_b16 v[86:87], v214 offset:21504\n"
    "v_mfma_f32_32x32x16_bf16 v[32:47], v[88:91], v[108:111], v[32:47]\n"
    "v_mfma_f32_32x32x16_bf16 v[48:63], v[92:95], v[104:107], v[48:63]\n"
    "v_mfma_f32_32x32x16_bf16 v[64:79], v[92:95], v[108:111], v[64:79]\n"
    "s_waitcnt vmcnt(16) lgkmcnt(0)\n"
    "v_mfma_f32_32x32x16_bf16 v[16:31], v[80:83], v[112:115], v[16:31]\n"
    "ds_read_b64_tr_b16 v[88:89], v213 offset:22528\n"
    "ds_read_b64_tr_b16 v[90:91], v213 offset:23552\n"
    "ds_read_b64_tr_b16 v[92:93], v214 offset:22528\n"
    "ds_read_b64_tr_b16 v[94:95], v214 offset:23552\n"
    "v_mfma_f32_32x32x16_bf16 v[32:47], v[80:83], v[116:119], v[32:47]\n"
    "v_mfma_f32_32x32x16_bf16 v[48:63], v[84:87], v[112:115], v[48:63]\n"
    "v_mfma_f32_32x32x16_bf16 v[64:79], v[84:87], v[116:119], v[64:79]\n"
    "s_waitcnt vmcnt(12) lgkmcnt(0)\n"
    "v_mfma_f32_32x32x16_bf16 v[16:31], v[88:91], v[120:123], v[16:31]\n"
    "v_mfma_f32_32x32x16_bf16 v[32:47], v[88:91], v[124:127], v[32:47]\n"
    "v_mfma_f32_32x32x16_bf16 v[48:63], v[92:95], v[120:123], v[48:63]\n"
    "v_mfma_f32_32x32x16_bf16 v[64:79], v[92:95], v[124:127], v[64:79]\n"
    "s_setprio 3\n"
    "v_add_u32 v205, 0x10000, v219\n"
    "v_xor_b32 v213, 64, v221\n"
    "s_nop 6\n"
    "ds_write_b128 v218, v[16:19] offset:0\n"
    "ds_write_b128 v218, v[20:23] offset:1024\n"
    "ds_write_b128 v218, v[24:27] offset:2048\n"
    "ds_write_b128 v218, v[28:31] offset:3072\n"
    "ds_write_b128 v218, v[32:35] offset:4096\n"
    "ds_write_b128 v218, v[36:39] offset:5120\n"
    "ds_write_b128 v218, v[40:43] offset:6144\n"
    "ds_write_b128 v218, v[44:47] offset:7168\n"
    "ds_write_b128 v218, v[48:51] offset:8192\n"
    "ds_write_b128 v218, v[52:55] offset:9216\n"
    "ds_write_b128 v218, v[56:59] offset:10240\n"
    "ds_write_b128 v218, v[60:63] offset:11264\n"
    "ds_write_b128 v218, v[64:67] offset:12288\n"
    "ds_write_b128 v218, v[68:71] offset:13312\n"
    "ds_write_b128 v218, v[72:75] offset:14336\n"
    "ds_write_b128 v218, v[76:79] offset:15360\n"
    "s_waitcnt vmcnt(0)\n"
    "s_mov_b64 exec, 1\n"
    "s_waitcnt lgkmcnt(0)\n"
    "ds_add_u32 v215, v216\n"
    "s_mov_b64 exec, -1\n"
    "s_mov_b32 s55, 0x400000\n"
    "12:\n"
    "ds_read_b32 v206, v215\n"
    "s_sub_u32 s55, s55, 1\n"
    "s_cbranch_scc1 13f\n"
    "s_waitcnt lgkmcnt(0)\n"
    "v_readfirstlane_b32 s51, v206\n"
    "s_sub_u32 s51, s51, 0x02020202\n"
    "s_and_b32 s51, s51, 0x80808080\n"
    "s_cbranch_scc1 12b\n"
    "13:\n"
    "ds_read_b128 v[48:51], v219 offset:0\n"
    "ds_read_b128 v[52:55], v219 offset:1024\n"
    "ds_read_b128 v[56:59], v219 offset:2048\n"
    "ds_read_b128 v[60:63], v219 offset:3072\n"
    "ds_read_b128 v[64:67], v219 offset:32768\n"
    "ds_read_b128 v[68:71], v219 offset:33792\n"
    "ds_read_b128 v[72:75], v219 offset:34816\n"
    "ds_read_b128 v[76:79], v219 offset:35840\n"
    "ds_read_b128 v[80:83], v205 offset:0\n"
    "ds_read_b128 v[84:87], v205 offset:1024\n"
    "ds_read_b128 v[88:91], v205 offset:2048\n"
    "ds_read_b128 v[92:95], v205 offset:3072\n"
    "ds_read_b128 v[96:99], v205 offset:32768\n"
    "ds_read_b128 v[100:103], v205 offset:33792\n"
    "ds_read_b128 v[104:107], v205 offset:34816\n"
    "ds_read_b128 v[108:111], v205 offset:35840\n"
    "v_lshlrev_b32 v16, 16, v192\n"
    "v_and_b32 v17, 0xffff0000, v192\n"
    "v_lshlrev_b32 v18, 16, v193\n"
    "v_and_b32 v19, 0xffff0000, v193\n"
    "v_lshlrev_b32 v20, 16, v194\n"
    "v_and_b32 v21, 0xffff0000, v194\n"
    "v_lshlrev_b32 v22, 16, v195\n"
    "v_and_b32 v23, 0xffff0000, v195\n"
    "v_lshlrev_b32 v24, 16, v196\n"
    "v_and_b32 v25, 0xffff0000, v196\n"
    "v_lshlrev_b32 v26, 16, v197\n"
    "v_and_b32 v27, 0xffff0000, v197\n"
    "v_lshlrev_b32 v28, 16, v198\n"
    "v_and_b32 v29, 0xffff0000, v198\n"
    "v_lshlrev_b32 v30, 16, v199\n"
    "v_and_b32 v31, 0xffff0000, v199\n"
    "s_waitcnt lgkmcnt(12)\n"
    "v_pk_add_f32 v[16:17], v[16:17], v[48:49]\n"
    "v_pk_add_f32 v[18:19], v[18:19], v[50:51]\n"
    "v_pk_add_f32 v[20:21], v[20:21], v[52:53]\n"
    "v_pk_add_f32 v[22:23], v[22:23], v[54:55]\n"
    "v_pk_add_f32 v[24:25], v[24:25], v[56:57]\n"
    "v_pk_add_f32 v[26:27], v[26:27], v[58:59]\n"
    "v_pk_add_f32 v[28:29], v[28:29], v[60:61]\n"
    "v_pk_add_f32 v[30:31], v[30:31], v[62:63]\n"
    "s_waitcnt lgkmcnt(8)\n"
    "v_pk_add_f32 v[16:17], v[16:17], v[64:65]\n"
    "v_pk_add_f32 v[18:19], v[18:19], v[66:67]\n"
    "v_pk_add_f32 v[20:21], v[20:21], v[68:69]\n"
    "v_pk_add_f32 v[22:23], v[22:23], v[70:71]\n"
    "v_pk_add_f32 v[24:25], v[24:25], v[72:73]\n"
    "v_pk_add_f32 v[26:27], v[26:27], v[74:75]\n"
    "v_pk_add_f32 v[28:29], v[28:29], v[76:77]\n"
    "v_pk_add_f32 v[30:31], v[30:31], v[78:79]\n"
    "s_waitcnt lgkmcnt(4)\n"
    "v_pk_add_f32 v[16:17], v[16:17], v[80:81]\n"
    "v_pk_add_f32 v[18:19], v[18:19], v[82:83]\n"
    "v_pk_add_f32 v[20:21], v[20:21], v[84:85]\n"
    "v_pk_add_f32 v[22:23], v[22:23], v[86:87]\n"
    "v_pk_add_f32 v[24:25], v[24:25], v[88:89]\n"
    "v_pk_add_f32 v[26:27], v[26:27], v[90:91]\n"
    "v_pk_add_f32 v[28:29], v[28:29], v[92:93]\n"
    "v_pk_add_f32 v[30:31], v[30:31], v[94:95]\n"
    "s_waitcnt lgkmcnt(0)\n"
    "v_pk_add_f32 v[16:17], v[16:17], v[96:97]\n"
    "v_pk_add_f32 v[18:19], v[18:19], v[98:99]\n"
    "v_pk_add_f32 v[20:21], v[20:21], v[100:101]\n"
    "v_pk_add_f32 v[22:23], v[22:23], v[102:103]\n"
    "v_pk_add_f32 v[24:25], v[24:25], v[104:105]\n"
    "v_pk_add_f32 v[26:27], v[26:27], v[106:107]\n"
    "v_pk_add_f32 v[28:29], v[28:29], v[108:109]\n"
    "v_pk_add_f32 v[30:31], v[30:31], v[110:111]\n"
    "s_mov_b64 exec, 1\n"
    "s_waitcnt lgkmcnt(0)\n"
    "ds_add_u32 v215, v216\n"
    "s_mov_b64 exec, -1\n"
    "ds_read_b64_tr_b16 v[48:49], v221 offset:24576\n"
    "ds_read_b64_tr_b16 v[50:51], v221 offset:25600\n"
    "ds_read_b64_tr_b16 v[52:53], v213 offset:24576\n"
    "ds_read_b64_tr_b16 v[54:55], v213 offset:25600\n"
    "ds_read_b64_tr_b16 v[56:57], v221 offset:26624\n"
    "ds_read_b64_tr_b16 v[58:59], v221 offset:27648\n"
    "ds_read_b64_tr_b16 v[60:61], v213 offset:26624\n"
    "ds_read_b64_tr_b16 v[62:63], v213 offset:27648\n"
    "ds_read_b64_tr_b16 v[64:65], v221 offset:28672\n"
    "ds_read_b64_tr_b16 v[66:67], v221 offset:29696\n"
    "ds_read_b64_tr_b16 v[68:69], v213 offset:28672\n"
    "ds_read_b64_tr_b16 v[70:71], v213 offset:29696\n"
    "ds_read_b64_tr_b16 v[72:73], v221 offset:30720\n"
    "ds_read_b64_tr_b16 v[74:75], v221 offset:31744\n"
    "ds_read_b64_tr_b16 v[76:77], v213 offset:30720\n"
    "ds_read_b64_tr_b16 v[78:79], v213 offset:31744\n"
    "ds_read_b64_tr_b16 v[80:81], v221 offset:0\n"
    "ds_read_b64_tr_b16 v[82:83], v221 offset:1024\n"
    "ds_read_b64_tr_b16 v[84:85], v213 offset:0\n"
    "ds_read_b64_tr_b16 v[86:87], v213 offset:1024\n"
    "ds_read_b64_tr_b16 v[88:89], v221 offset:2048\n"
    "ds_read_b64_tr_b16 v[90:91], v221 offset:3072\n"
    "ds_read_b64_tr_b16 v[92:93], v213 offset:2048\n"
    "ds_read_b64_tr_b16 v[94:95], v213 offset:3072\n"
    "ds_read_b64_tr_b16 v[96:97], v221 offset:4096\n"
    "ds_read_b64_tr_b16 v[98:99], v221 offset:5120\n"
    "ds_read_b64_tr_b16 v[100:101], v213 offset:4096\n"
    "ds_read_b64_tr_b16 v[102:103], v213 offset:5120\n"
    "ds_read_b64_tr_b16 v[104:105], v221 offset:6144\n"
    "ds_read_b64_tr_b16 v[106:107], v221 offset:7168\n"
    "ds_read_b64_tr_b16 v[108:109], v213 offset:6144\n"
    "ds_read_b64_tr_b16 v[110:111], v213 offset:7168\n"
    "s_mov_b32 s55, 0x400000\n"
    "14:\n"
    "ds_read_b32 v206, v215\n"
    "s_sub_u32 s55, s55, 1\n"
    "s_cbranch_scc1 15f\n"
    "s_waitcnt lgkmcnt(0)\n"
    "v_readfirstlane_b32 s51, v206\n"
    "s_sub_u32 s51, s51, 0x03030303\n"
    "s_and_b32 s51, s51, 0x80808080\n"
    "s_cbranch_scc1 14b\n"
    "15:\n"
    "s_mov_b32 s55, 0x40000\n"
    "16:\n"
    "global_load_dwordx2 v[204:205], v220, s[70:71] sc1\n"
    "s_sub_u32 s55, s55, 1\n"
    "s_cbranch_scc1 17f\n"
    "s_waitcnt vmcnt(0)\n"
    "v_cmp_ne_u64 vcc, s[72:73], v[204:205]\n"
    "s_cbranch_vccnz 16b\n"
    "17:\n"
    "global_load_dwordx4 v[112:115], v212, s[82:83] offset:-4096 sc1\n"
    "global_load_dwordx4 v[116:119], v212, s[82:83] offset:-3072 sc1\n"
    "global_load_dwordx4 v[120:123], v212, s[82:83] offset:-2048 sc1\n"
    "global_load_dwordx4 v[124:127], v212, s[82:83] offset:-1024 sc1\n"
    "global_load_dwordx4 v[128:131], v212, s[82:83] offset:0 sc1\n"
    "global_load_dwordx4 v[132:135], v212, s[82:83] offset:1024 sc1\n"
    "global_load_dwordx4 v[136:139], v212, s[82:83] offset:2048 sc1\n"
    "global_load_dwordx4 v[140:143], v212, s[82:83] offset:3072 sc1\n"
    "s_add_u32 s82, s82, 8192\n"
    "s_addc_u32 s83, s83, 0\n"
    "global_load_dwordx4 v[144:147], v212, s[82:83] offset:-4096 sc1\n"
    "global_load_dwordx4 v[148:151], v212, s[82:83] offset:-3072 sc1\n"
    "global_load_dwordx4 v[152:155], v212, s[82:83] offset:-2048 sc1\n"
    "global_load_dwordx4 v[156:159], v212, s[82:83] offset:-1024 sc1\n"
    "global_load_dwordx4 v[160:163], v212, s[82:83] offset:0 sc1\n"
    "global_load_dwordx4 v[164:167], v212, s[82:83] offset:1024 sc1\n"
    "global_load_dwordx4 v[168:171], v212, s[82:83] offset:2048 sc1\n"
    "global_load_dwordx4 v[172:175], v212, s[82:83] offset:3072 sc1\n"
    "s_waitcnt vmcnt(15)\n"
    "v_mfma_f32_32x32x16_bf16 v[32:47], v[52:55], v[112:115], 0\n"
    "v_mfma_f32_32x32x16_bf16 v[16:31], v[48:51], v[112:115], v[16:31]\n"
    "ds_read_b64_tr_b16 v[48:49], v221 offset:57344\n"
    "ds_read_b64_tr_b16 v[50:51], v221 offset:58368\n"
    "ds_read_b64_tr_b16 v[52:53], v213 offset:57344\n"
    "ds_read_b64_tr_b16 v[54:55], v213 offset:58368\n"
    "s_waitcnt vmcnt(14)\n"
    "v_mfma_f32_32x32x16_bf16 v[32:47], v[60:63], v[116:119], v[32:47]\n"
    "v_mfma_f32_32x32x16_bf16 v[16:31], v[56:59], v[116:119], v[16:31]\n"
    "ds_read_b64_tr_b16 v[56:57], v221 offset:59392\n"
    "ds_read_b64_tr_b16 v[58:59], v221 offset:60416\n"
    "ds_read_b64_tr_b16 v[60:61], v213 offset:59392\n"
    "ds_read_b64_tr_b16 v[62:63], v213 offset:60416\n"
    "s_waitcnt vmcnt(13)\n"
    "v_mfma_f32_32x32x16_bf16 v[32:47], v[68:71], v[120:123], v[32:47]\n"
    "v_mfma_f32_32x32x16_bf16 v[16:31], v[64:67], v[120:123], v[16:31]\n"
    "ds_read_b64_tr_b16 v[64:65], v221 offset:61440\n"
    "ds_read_b64_tr_b16 v[66:67], v221 offset:62464\n"
    "ds_read_b64_tr_b16 v[68:69], v213 offset:61440\n"
    "ds_read_b64_tr_b16 v[70:71], v213 offset:62464\n"
    "s_waitcnt vmcnt(12)\n"
    "v_mfma_f32_32x32x16_bf16 v[32:47], v[76:79], v[124:127], v[32:47]\n"
    "v_mfma_f32_32x32x16_bf16 v[16:31], v[72:75], v[124:127], v[16:31]\n"
    "ds_read_b64_tr_b16 v[72:73], v221 offset:63488\n"
    "ds_read_b64_tr_b16 v[74:75], v221 offset:64512\n"
    "ds_read_b64_tr_b16 v[76:77], v213 offset:63488\n"
    "ds_read_b64_tr_b16 v[78:79], v213 offset:64512\n"
    "s_waitcnt vmcnt(11)\n"
    "v_mfma_f32_32x32x16_bf16 v[32:47], v[84:87], v[128:131], v[32:47]\n"
    "v_mfma_f32_32x32x16_bf16 v[16:31], v[80:83], v[128:131], v[16:31]\n"
    "ds_read_b64_tr_b16 v[80:81], v221 offset:32768\n"
    "ds_read_b64_tr_b16 v[82:83], v221 offset:33792\n"
    "ds_read_b64_tr_b16 v[84:85], v213 offset:32768\n"
    "ds_read_b64_tr_b16 v[86:87], v213 offset:33792\n"
    "s_waitcnt vmcnt(10)\n"
    "v_mfma_f32_32x32x16_bf16 v[32:47], v[92:95], v[132:135], v[32:47]\n"
    "v_mfma_f32_32x32x16_bf16 v[16:31], v[88:91], v[132:135], v[16:31]\n"
    "ds_read_b64_tr_b16 v[88:89], v221 offset:34816\n"
    "ds_read_b64_tr_b16 v[90:91], v221 offset:35840\n"
    "ds_read_b64_tr_b16 v[92:93], v213 offset:34816\n"
    "ds_read_b64_tr_b16 v[94:95], v213 offset:35840\n"
    "s_waitcnt vmcnt(9)\n"
    "v_mfma_f32_32x32x16_bf16 v[32:47], v[100:103], v[136:139], v[32:47]\n"
    "v_mfma_f32_32x32x16_bf16 v[16:31], v[96:99], v[136:139], v[16:31]\n"
    "ds_read_b64_tr_b16 v[96:97], v221 offset:36864\n"
    "ds_read_b64_tr_b16 v[98:99], v221 offset:37888\n"
    "ds_read_b64_tr_b16 v[100:101], v213 offset:36864\n"
    "ds_read_b64_tr_b16 v[102:103], v213 offset:37888\n"
    "s_waitcnt vmcnt(8)\n"
    "v_mfma_f32_32x32x16_bf16 v[32:47], v[108:111], v[140:143], v[32:47]\n"
    "v_mfma_f32_32x32x16_bf16 v[16:31], v[104:107], v[140:143], v[16:31]\n"
    "ds_read_b64_tr_b16 v[104:105], v221 offset:38912\n"
    "ds_read_b64_tr_b16 v[106:107], v221 offset:39936\n"
    "ds_read_b64_tr_b16 v[108:109], v213 offset:38912\n"
    "ds_read_b64_tr_b16 v[110:111], v213 offset:39936\n"
    "s_waitcnt vmcnt(7) lgkmcnt(12)\n"
    "v_mfma_f32_32x32x16_bf16 v[32:47], v[52:55], v[144:147], v[32:47]\n"
    "v_mfma_f32_32x32x16_bf16 v[16:31], v[48:51], v[144:147], v[16:31]\n"
    "s_waitcnt vmcnt(6) lgkmcnt(12)\n"
    "v_mfma_f32_32x32x16_bf16 v[32:47], v[60:63], v[148:151], v[32:47]\n"
    "v_mfma_f32_32x32x16_bf16 v[16:31], v[56:59], v[148:151], v[16:31]\n"
    "s_waitcnt vmcnt(5) lgkmcnt(12)\n"
    "v_mfma_f32_32x32x16_bf16 v[32:47], v[68:71], v[152:155], v[32:47]\n"
    "v_mfma_f32_32x32x16_bf16 v[16:31], v[64:67], v[152:155], v[16:31]\n"
    "s_waitcnt vmcnt(4) lgkmcnt(12)\n"
    "v_mfma_f32_32x32x16_bf16 v[32:47], v[76:79], v[156:159], v[32:47]\n"
    "v_mfma_f32_32x32x16_bf16 v[16:31], v[72:75], v[156:159], v[16:31]\n"
    "s_waitcnt vmcnt(3) lgkmcnt(0)\n"
    "v_mfma_f32_32x32x16_bf16 v[32:47], v[84:87], v[160:163], v[32:47]\n"
    "s_waitcnt vmcnt(2)\n"
    "v_mfma_f32_32x32x16_bf16 v[32:47], v[92:95], v[164:167], v[32:47]\n"
    "s_waitcnt vmcnt(1)\n"
    "v_mfma_f32_32x32x16_bf16 v[32:47], v[100:103], v[168:171], v[32:47]\n"
    "s_waitcnt vmcnt(0)\n"
    "v_mfma_f32_32x32x16_bf16 v[32:47], v[108:111], v[172:175], v[32:47]\n"
    "v_mfma_f32_32x32x16_bf16 v[16:31], v[80:83], v[160:163], v[16:31]\n"
    "v_mfma_f32_32x32x16_bf16 v[16:31], v[88:91], v[164:167], v[16:31]\n"
    "v_mfma_f32_32x32x16_bf16 v[16:31], v[96:99], v[168:171], v[16:31]\n"
    "s_nop 8\n"
    "ds_write_b128 v218, v[32:35] offset:0\n"
    "ds_write_b128 v218, v[36:39] offset:1024\n"
    "ds_write_b128 v218, v[40:43] offset:2048\n"
    "ds_write_b128 v218, v[44:47] offset:3072\n"
    "v_mfma_f32_32x32x16_bf16 v[16:31], v[104:107], v[172:175], v[16:31]\n"
    "s_mov_b64 exec, 1\n"
    "s_waitcnt lgkmcnt(0)\n"
    "ds_add_u32 v215, v216\n"
    "s_mov_b64 exec, -1\n"
    "s_mov_b32 s55, 0x400000\n"
    "18:\n"
    "ds_read_b32 v206, v215\n"
    "s_sub_u32 s55, s55, 1\n"
    "s_cbranch_scc1 19f\n"
    "s_waitcnt lgkmcnt(0)\n"
    "v_readfirstlane_b32 s51, v206\n"
    "s_and_b32 s51, s51, s84\n"
    "s_cbranch_scc0 18b\n"
    "19:\n"
    "ds_read_b128 v[32:35], v222 offset:0\n"
    "ds_read_b128 v[36:39], v222 offset:1024\n"
    "ds_read_b128 v[40:43], v222 offset:2048\n"
    "ds_read_b128 v[44:47], v222 offset:3072\n"
    "s_waitcnt lgkmcnt(3)\n"
    "v_pk_add_f32 v[16:17], v[16:17], v[32:33]\n"
    "v_pk_add_f32 v[18:19], v[18:19], v[34:35]\n"
    "s_waitcnt lgkmcnt(2)\n"
    "v_pk_add_f32 v[20:21], v[20:21], v[36:37]\n"
    "v_pk_add_f32 v[22:23], v[22:23], v[38:39]\n"
    "s_waitcnt lgkmcnt(1)\n"
    "v_pk_add_f32 v[24:25], v[24:25], v[40:41]\n"
    "v_pk_add_f32 v[26:27], v[26:27], v[42:43]\n"
    "s_waitcnt lgkmcnt(0)\n"
    "v_pk_add_f32 v[28:29], v[28:29], v[44:45]\n"
    "v_pk_add_f32 v[30:31], v[30:31], v[46:47]\n"
    "v_cvt_pk_bf16_f32 v48, v16, v17\n"
    "v_cvt_pk_bf16_f32 v49, v18, v19\n"
    "v_cvt_pk_bf16_f32 v50, v20, v21\n"
    "v_cvt_pk_bf16_f32 v51, v22, v23\n"
    "v_cvt_pk_bf16_f32 v52, v24, v25\n"
    "v_cvt_pk_bf16_f32 v53, v26, v27\n"
    "v_cvt_pk_bf16_f32 v54, v28, v29\n"
    "v_cvt_pk_bf16_f32 v55, v30, v31\n"
    "v_permlane32_swap_b32 v48, v50\n"
    "v_permlane32_swap_b32 v49, v51\n"
    "v_permlane32_swap_b32 v52, v54\n"
    "v_permlane32_swap_b32 v53, v55\n"
    "global_store_dwordx4 v210, v[48:51], s[80:81] sc1\n"
    "global_store_dwordx4 v210, v[52:55], s[80:81] offset:32 sc1\n"
            : "+{v[208:223]}"(vi), "+{s[64:79]}"(si), "+{s[80:87]}"(sj) :: "memory", "scc", "vcc", "v16", "v17", "v18", "v19", "v20", "v21", "v22", "v23", "v24", "v25", "v26", "v27", "v28", "v29", "v30", "v31", "v32", "v33", "v34", "v35", "v36", "v37", "v38", "v39", "v40", "v41", "v42", "v43", "v44", "v45", "v46", "v47", "v48", "v49", "v50", "v51", "v52", "v53", "v54", "v55", "v56", "v57", "v58", "v59", "v60", "v61", "v62", "v63", "v64", "v65", "v66", "v67", "v68", "v69", "v70", "v71", "v72", "v73", "v74", "v75", "v76", "v77", "v78", "v79", "v80", "v81", "v82", "v83", "v84", "v85", "v86", "v87", "v88", "v89", "v90", "v91", "v92", "v93", "v94", "v95", "v96", "v97", "v98", "v99", "v100", "v101", "v102", "v103", "v104", "v105", "v106", "v107", "v108", "v109", "v110", "v111", "v112", "v113", "v114", "v115", "v116", "v117", "v118", "v119", "v120", "v121", "v122", "v123", "v124", "v125", "v126", "v127", "v128", "v129", "v130", "v131", "v132", "v133", "v134", "v135", "v136", "v137", "v138", "v139", "v140", "v141", "v142", "v143", "v144", "v145", "v146", "v147", "v148", "v149", "v150", "v151", "v152", "v153", "v154", "v155", "v156", "v157", "v158", "v159", "v160", "v161", "v162", "v163", "v164", "v165", "v166", "v167", "v168", "v169", "v170", "v171", "v172", "v173", "v174", "v175", "v176", "v177", "v178", "v179", "v180", "v181", "v182", "v183", "v184", "v185", "v186", "v187", "v188", "v189", "v190", "v191", "v192", "v193", "v194", "v195", "v196", "v197", "v198", "v199", "v200", "v201", "v202", "v203", "v204", "v205", "v206", "v207", "s51", "s55", "s62", "s63");
    }
    STAMP(10); LDUMP();
}

extern "C" void kernel_launch(void* const* d_in, const int* in_sizes, int n_in, void* d_out, int out_size, void* d_ws, size_t ws_size, hipStream_t stream) {
    fused_block<<<256, 512, 0, stream>>>((const char*)d_in[0], (const char*)d_in[1], (const char*)d_in[2], (const char*)d_in[4], (const char*)d_in[5], (const char*)d_in[6], (char*)d_out, (char*)d_ws);
}
```
